```python
import torch

def _softmax(x, axis=-1):
    return torch.softmax(x, dim=axis)
def _gelu(x):
    return torch.nn.functional.gelu(x, approximate="tanh")
def _silu(x):
    return torch.nn.functional.silu(x)
def _sigmoid(x):
    return torch.sigmoid(x)
def _layernorm(x, axis=-1, eps=1e-5):
    m = x.mean(dim=axis, keepdim=True)
    v = x.var(dim=axis, keepdim=True, unbiased=False)
    return (x - m) / torch.sqrt(v + eps)
def _scatter_add(tgt, idx, src, axis=0):
    return tgt.clone().index_add_(axis, idx, src)
def _segment_sum(src, seg, n):
    out = torch.zeros((n,) + src.shape[1:], dtype=src.dtype)
    return out.index_add_(0, seg, src)
def _pad(x, pw):
    flat = tuple(v for pair in reversed(pw) for v in pair)
    return torch.nn.functional.pad(x, flat)

kernel_name = 'concat_heads__mha_block_cached__gqa_block_cached__near_ridge'

def setup_inputs():

    _w = (lambda a: (lambda t: t.to(torch.bfloat16) if t.is_floating_point() else t)(torch.as_tensor(a)))
    def _A_setup():
        B, Sq, D = 16, 128, 512
        H, Sk, Dh = 32, 512, 128
        return {
            "x": _w((lambda *s: torch.randn(*s, dtype=torch.bfloat16) * 0.8)(B, Sq, D)),
            "k_cache": _w((lambda *s: torch.randn(*s, dtype=torch.bfloat16) * 0.8)(B, H, Sk, Dh)),
            "v_cache": _w((lambda *s: torch.randn(*s, dtype=torch.bfloat16) * 0.05)(B, H, Sk, Dh)),
            "wq": _w((lambda *s: torch.randn(*s, dtype=torch.bfloat16) * 0.8)(D, H * Dh) * (D ** -0.5)),
            "wo": _w((lambda *s: torch.randn(*s, dtype=torch.bfloat16) * 0.05)(H * Dh, D) * ((H * Dh) ** -0.5)),
        }
    _A_in = _A_setup()

    def _B_setup():
        B, Sq, D = 16, 128, 512
        Hq, Hkv, Sk, Dh = 32, 32, 512, 128
        assert Hq % Hkv == 0, (Hq, Hkv)
        return {
            "x": _w((lambda *s: torch.randn(*s, dtype=torch.bfloat16) * 0.8)(B, Sq, D)),
            "k_cache": _w((lambda *s: torch.randn(*s, dtype=torch.bfloat16) * 0.8)(B, Hkv, Sk, Dh)),
            "v_cache": _w((lambda *s: torch.randn(*s, dtype=torch.bfloat16) * 0.05)(B, Hkv, Sk, Dh)),
            "wq": _w((lambda *s: torch.randn(*s, dtype=torch.bfloat16) * 0.8)(D, Hq * Dh) * (D ** -0.5)),
            "wo": _w((lambda *s: torch.randn(*s, dtype=torch.bfloat16) * 0.05)(Hq * Dh, D) * ((Hq * Dh) ** -0.5)),
        }
    _B_in = _B_setup()

    return {
        "A_x": _A_in["x"],
        "A_k_cache": _A_in["k_cache"],
        "A_v_cache": _A_in["v_cache"],
        "A_wq": _A_in["wq"],
        "A_wo": _A_in["wo"],
        "B_x": _B_in["x"],
        "B_k_cache": _B_in["k_cache"],
        "B_v_cache": _B_in["v_cache"],
        "B_wq": _B_in["wq"],
        "B_wo": _B_in["wo"],
    }

def reference(A_x, A_k_cache, A_v_cache, A_wq, A_wo, B_x, B_k_cache, B_v_cache, B_wq, B_wo):
    def _A_ref(x, k_cache, v_cache, wq, wo):
        B, Sq, D = x.shape
        H, Dh = 32, 128
        q = (lambda x, *d: x.permute(*d))((x @ wq).reshape(B, Sq, H, Dh), 0, 2, 1, 3)
        scores = torch.einsum("bhqd,bhkd->bhqk", q, k_cache) * (Dh ** -0.5)
        p = _softmax(scores, axis=-1)
        o = torch.einsum("bhqk,bhkd->bhqd", p, v_cache)
        return (lambda x, *d: x.permute(*d))(o, 0, 2, 1, 3).reshape(B, Sq, H * Dh) @ wo

    def _B_ref(x, k_cache, v_cache, wq, wo):
        B, Sq, D = x.shape
        Hq, Dh = 32, 128
        Hkv = k_cache.shape[1]
        rep = Hq // Hkv
        q = (lambda x, *d: x.permute(*d))((x @ wq).reshape(B, Sq, Hq, Dh), 0, 2, 1, 3)
        k = (lambda x, n, axis: torch.repeat_interleave(x, n, dim=axis))(k_cache, rep, axis=1)
        v = (lambda x, n, axis: torch.repeat_interleave(x, n, dim=axis))(v_cache, rep, axis=1)
        scores = torch.einsum("bhqd,bhkd->bhqk", q, k) * (Dh ** -0.5)
        p = _softmax(scores, axis=-1)
        o = torch.einsum("bhqk,bhkd->bhqd", p, v)
        return (lambda x, *d: x.permute(*d))(o, 0, 2, 1, 3).reshape(B, Sq, Hq * Dh) @ wo

    a = _A_ref(A_x, A_k_cache, A_v_cache, A_wq, A_wo)
    b = _B_ref(B_x, B_k_cache, B_v_cache, B_wq, B_wo)
    return torch.concatenate([a, b], axis=-1)
```

```cpp
#include <hip/hip_runtime.h>
#include <hip/hip_bf16.h>
#include <stdint.h>

typedef __attribute__((ext_vector_type(8))) __bf16 bf16x8;
typedef __attribute__((ext_vector_type(4))) __bf16 bf16x4;
typedef __attribute__((ext_vector_type(2))) __bf16 bf16x2;
typedef __attribute__((ext_vector_type(16))) float f32x16;
typedef __attribute__((ext_vector_type(2))) float f32x2;
typedef __attribute__((ext_vector_type(4))) unsigned u32x4;
#define LAS __attribute__((address_space(3)))
#define DI __device__ __forceinline__
typedef __amdgpu_buffer_rsrc_t rsrc_t;

constexpr int NB = 16, SQ = 128, DM = 512, NH = 32, SK = 512, DH = 128;
constexpr unsigned SLOT = 16384, NSLOT = 5;

#define WAITV(n) asm volatile("s_waitcnt vmcnt(" #n ")" ::: "memory")
#define BARRIER() asm volatile("s_waitcnt lgkmcnt(0)\n\ts_barrier" ::: "memory")

DI void dma16(unsigned m0v, unsigned voff, rsrc_t r, unsigned soff) {
  asm volatile("s_mov_b32 m0, %0\n\ts_nop 0\n\tbuffer_load_dwordx4 %1, %2, %3 offen lds"
               :: "s"(m0v), "v"(voff), "s"(r), "s"(soff) : "memory");
}
DI void dma16nt(unsigned m0v, unsigned voff, rsrc_t r, unsigned soff) {
  asm volatile("s_mov_b32 m0, %0\n\ts_nop 0\n\tbuffer_load_dwordx4 %1, %2, %3 offen nt lds"
               :: "s"(m0v), "v"(voff), "s"(r), "s"(soff) : "memory");
}
DI unsigned cvtpk(float lo, float hi) {
  f32x2 v = {lo, hi};
  bf16x2 b = __builtin_convertvector(v, bf16x2);
  return __builtin_bit_cast(unsigned, b);
}
DI bf16x8 pack8(const f32x16& a, int base, float sc) {
  u32x4 w = {cvtpk(a[base + 0] * sc, a[base + 1] * sc), cvtpk(a[base + 2] * sc, a[base + 3] * sc),
             cvtpk(a[base + 4] * sc, a[base + 5] * sc), cvtpk(a[base + 6] * sc, a[base + 7] * sc)};
  return __builtin_bit_cast(bf16x8, w);
}
DI bf16x8 pack8n(const f32x16& a, int base) {
  u32x4 w = {cvtpk(a[base + 0], a[base + 1]), cvtpk(a[base + 2], a[base + 3]),
             cvtpk(a[base + 4], a[base + 5]), cvtpk(a[base + 6], a[base + 7])};
  return __builtin_bit_cast(bf16x8, w);
}
DI bf16x8 lds_b128(unsigned addr) { return *(LAS const bf16x8*)(addr); }
DI bf16x8 lds_tr2(unsigned addr0, unsigned addr1) {
  bf16x4 lo = __builtin_amdgcn_ds_read_tr16_b64_v4bf16((LAS bf16x4*)(addr0));
  bf16x4 hi = __builtin_amdgcn_ds_read_tr16_b64_v4bf16((LAS bf16x4*)(addr1));
  return __builtin_shufflevector(lo, hi, 0, 1, 2, 3, 4, 5, 6, 7);
}
DI float xhalf_max(float x) {
  auto r = __builtin_amdgcn_permlane32_swap(__float_as_uint(x), __float_as_uint(x), false, false);
  return fmaxf(__uint_as_float(r[0]), __uint_as_float(r[1]));
}
DI float xhalf_sum(float x) {
  auto r = __builtin_amdgcn_permlane32_swap(__float_as_uint(x), __float_as_uint(x), false, false);
  return __uint_as_float(r[0]) + __uint_as_float(r[1]);
}
#define MFMA(a, b, c) __builtin_amdgcn_mfma_f32_32x32x16_bf16((a), (b), (c), 0, 0, 0)

__global__ __launch_bounds__(256, 2) void attn_kernel(
    const __bf16* __restrict__ xA, const __bf16* __restrict__ kA, const __bf16* __restrict__ vA, const __bf16* __restrict__ wqA,
    const __bf16* __restrict__ xB, const __bf16* __restrict__ kB, const __bf16* __restrict__ vB, const __bf16* __restrict__ wqB,
    __bf16* __restrict__ ows) {
  extern __shared__ __attribute__((aligned(16))) char smem[];
  const unsigned ldsb = (unsigned)(size_t)(LAS char*)smem;
  const unsigned tid = threadIdx.x, lane = tid & 63u;
  const unsigned w = __builtin_amdgcn_readfirstlane(tid >> 6);
  const unsigned bid = blockIdx.x;
  const unsigned xcd = bid & 7u, idx = bid >> 3;
  const unsigned br = xcd >> 2, h = (xcd & 3u) * 8u + (idx & 7u), b = idx >> 3;
  const __bf16* x = (br ? xB : xA) + (size_t)b * SQ * DM;
  const __bf16* wq = (br ? wqB : wqA) + (size_t)h * DH;
  const __bf16* kc = (br ? kB : kA) + ((size_t)b * NH + h) * SK * DH;
  const __bf16* vc = (br ? vB : vA) + ((size_t)b * NH + h) * SK * DH;
  const rsrc_t rx = __builtin_amdgcn_make_buffer_rsrc((void*)x, 0, SQ * DM * 2, 0x00020000);
  const rsrc_t rw = __builtin_amdgcn_make_buffer_rsrc((void*)wq, 0, ((DM - 1) * NH * DH + DH) * 2, 0x00020000);
  const rsrc_t rk = __builtin_amdgcn_make_buffer_rsrc((void*)kc, 0, SK * DH * 2, 0x00020000);
  const rsrc_t rv = __builtin_amdgcn_make_buffer_rsrc((void*)vc, 0, SK * DH * 2, 0x00020000);

  const unsigned l4 = lane >> 4, l15 = lane & 15u;
  const unsigned voffK = (4u * w + l4) * 256u + ((l15 ^ (4u * w + l4)) << 4);
  const unsigned voffV = (4u * w + l4) * 256u + ((l15 ^ (l4 << 2)) << 4);
  const unsigned voffW = (4u * w + l4) * 8192u + ((l15 ^ (l4 << 2)) << 4);
  const unsigned voffX = (8u * w + (lane >> 3)) * 1024u + (((lane & 7u) ^ ((4u * w + l4) & 7u)) << 4);
  const unsigned r32 = lane & 31u, hh = lane >> 5;
  const unsigned q4 = (lane >> 2) & 3u, p4 = lane & 3u, dhalf = (lane >> 4) & 1u;
  const unsigned lbK = ldsb + r32 * 256u + ((hh ^ (r32 & 15u)) << 4);
  const unsigned lbV = ldsb + (4u * hh + q4) * 256u + (q4 << 6) + 32u * dhalf + 8u * p4;
  const unsigned psw = ((p4 & 1u) << 1) | (p4 >> 1);
  const unsigned lbW = ldsb + (8u * hh + q4) * 256u + (q4 << 6) + 32u * dhalf + 8u * psw;
  const unsigned lbX = ldsb + (32u * w + r32) * 128u + ((hh ^ ((r32 >> 1) & 7u)) << 4);

  auto issue = [&](unsigned m, unsigned slot) {
    const unsigned m0b = ldsb + slot * SLOT + w * 1024u;
    if (m < 16u) {
      const unsigned j = m >> 1;
      if ((m & 1u) == 0) {
#pragma unroll
        for (unsigned i = 0; i < 4; ++i) dma16(m0b + i * 4096u, voffX, rx, j * 128u + i * 32768u);
      } else {
#pragma unroll
        for (unsigned i = 0; i < 4; ++i) dma16(m0b + i * 4096u, voffW, rw, j * 64u * 8192u + i * 16u * 8192u);
      }
    } else {
      const unsigned t = (m - 16u) >> 1;
      if ((m & 1u) == 0) {
#pragma unroll
        for (unsigned i = 0; i < 4; ++i) dma16nt(m0b + i * 4096u, voffK, rk, t * 16384u + i * 4096u);
      } else {
#pragma unroll
        for (unsigned i = 0; i < 4; ++i) dma16nt(m0b + i * 4096u, voffV, rv, t * 16384u + i * 4096u);
      }
    }
  };
  auto nxt = [](unsigned s) { return s == NSLOT - 1 ? 0u : s + 1u; };

  issue(0, 0); issue(1, 1); issue(2, 2); issue(3, 3); issue(4, 4);
  unsigned cs = 0;
  unsigned is = 0;

  f32x16 acc[4] = {};
  for (unsigned j = 0; j < 8; ++j) {
    if (j == 0) WAITV(12); else WAITV(4);
    BARRIER();
    if (j > 0) {
      issue(2 * j + 3, is); is = nxt(is);
      issue(2 * j + 4, is); is = nxt(is);
    }
    const unsigned sx = cs * SLOT; cs = nxt(cs);
    const unsigned sw = cs * SLOT; cs = nxt(cs);
    const unsigned ax = lbX + sx, aw = lbW + sw;
#pragma unroll
    for (unsigned ks = 0; ks < 4; ++ks) {
      bf16x8 xb = lds_b128(ax ^ (ks << 5));
#pragma unroll
      for (unsigned db = 0; db < 4; ++db) {
        const unsigned a = (aw ^ (db << 6)) + 4096u * ks;
        bf16x8 af = lds_tr2(a, a + 1024u);
        acc[db] = MFMA(af, xb, acc[db]);
      }
    }
  }
  bf16x8 qf[8];
#pragma unroll
  for (int db = 0; db < 4; ++db) { qf[2 * db] = pack8n(acc[db], 0); qf[2 * db + 1] = pack8n(acc[db], 8); }

  constexpr float C = 0.08838834764831845f * 1.4426950408889634f;
  constexpr float THRS = 8.0f / C;
  f32x16 o[4] = {};
  float m = 0.f, l = 0.f;
  for (unsigned t = 0; t < 8; ++t) {
    if (t == 0) WAITV(8); else if (t == 7) WAITV(4); else WAITV(12);
    BARRIER();
    if (t == 0) { issue(19, is); is = nxt(is); }
    if (t < 6) { issue(20 + 2 * t, is); is = nxt(is); }
    const unsigned ak = lbK + cs * SLOT; cs = nxt(cs);
    f32x16 s0 = {}, s1 = {};
#pragma unroll
    for (unsigned s = 0; s < 8; ++s) {
      const unsigned a = ak ^ (s << 5);
      bf16x8 k0 = lds_b128(a), k1 = lds_b128(a + 8192u);
      s0 = MFMA(k0, qf[s], s0);
      s1 = MFMA(k1, qf[s], s1);
    }
    float tmax = fmaxf(s0[0], s1[0]);
#pragma unroll
    for (int r = 1; r < 16; ++r) tmax = fmaxf(tmax, fmaxf(s0[r], s1[r]));
    tmax = xhalf_max(tmax);
    if (t == 0) {
      m = tmax;
    } else if (__any(tmax - m > THRS)) {
      const float mn = fmaxf(m, tmax);
      const float alpha = __builtin_amdgcn_exp2f((m - mn) * C);
      m = mn; l *= alpha;
#pragma unroll
      for (int db = 0; db < 4; ++db)
#pragma unroll
        for (int r = 0; r < 16; ++r) o[db][r] *= alpha;
    }
    const float mc = -m * C;
    float ps = 0.f;
#pragma unroll
    for (int r = 0; r < 16; ++r) {
      s0[r] = __builtin_amdgcn_exp2f(fmaf(s0[r], C, mc));
      s1[r] = __builtin_amdgcn_exp2f(fmaf(s1[r], C, mc));
      ps += s0[r] + s1[r];
    }
    l += ps;
    bf16x8 pf[4];
    pf[0] = pack8n(s0, 0); pf[1] = pack8n(s0, 8); pf[2] = pack8n(s1, 0); pf[3] = pack8n(s1, 8);
    if (t == 6) WAITV(8); else if (t == 7) WAITV(0); else WAITV(12);
    BARRIER();
    if (t < 6) { issue(21 + 2 * t, is); is = nxt(is); }
    const unsigned av = lbV + cs * SLOT; cs = nxt(cs);
#pragma unroll
    for (unsigned ks = 0; ks < 4; ++ks) {
#pragma unroll
      for (unsigned db = 0; db < 4; ++db) {
        const unsigned a = (av ^ (db << 6)) + 4096u * ks;
        bf16x8 vf = lds_tr2(a, a + 2048u);
        o[db] = MFMA(vf, pf[ks], o[db]);
      }
    }
  }
  const float inv = 1.0f / xhalf_sum(l);
  const unsigned u = (br * NB + b) * NH + h;
  bf16x8* op = (bf16x8*)ows + ((size_t)(u * 4u + w) * 8u) * 64u + lane;
#pragma unroll
  for (int db = 0; db < 4; ++db) {
    op[(2 * db) * 64] = pack8(o[db], 0, inv);
    op[(2 * db + 1) * 64] = pack8(o[db], 8, inv);
  }
}

DI void ldo(bf16x8& d, unsigned voff, const void* base, const int imm) {
  asm volatile("global_load_dwordx4 %0, %1, %2 offset:%3" : "=v"(d) : "v"(voff), "s"(base), "i"(imm) : "memory");
}
#define LDO8(S, base)                                                                     \
  do {                                                                                    \
    ldo(S[0], vo, base, 0); ldo(S[1], vo, base, 1024); ldo(S[2], vo, base, 2048);         \
    ldo(S[3], vo, base, 3072); ldo(S[4], vo2, base, 0); ldo(S[5], vo2, base, 1024);       \
    ldo(S[6], vo2, base, 2048); ldo(S[7], vo2, base, 3072);                               \
  } while (0)
#define WAITO(n, S)                                                                       \
  asm volatile("s_waitcnt vmcnt(" #n ")"                                                  \
               : "+v"(S[0]), "+v"(S[1]), "+v"(S[2]), "+v"(S[3]), "+v"(S[4]), "+v"(S[5]),  \
                 "+v"(S[6]), "+v"(S[7])::"memory")

__global__ __launch_bounds__(256, 1) void oproj_kernel(const __bf16* __restrict__ ows, const __bf16* __restrict__ woA,
                                                       const __bf16* __restrict__ woB, __bf16* __restrict__ out) {
  extern __shared__ __attribute__((aligned(16))) char smem[];
  const unsigned ldsb = (unsigned)(size_t)(LAS char*)smem;
  const unsigned tid = threadIdx.x, lane = tid & 63u;
  const unsigned w = __builtin_amdgcn_readfirstlane(tid >> 6);
  const unsigned bid = blockIdx.x;
  const unsigned xcd = bid & 7u, idx = bid >> 3;
  const unsigned pair = xcd * 4u + (idx >> 3), nt = idx & 7u;
  const unsigned br = pair >> 4, b = pair & 15u;
  const __bf16* wo = (br ? woB : woA) + nt * 64u;
  const rsrc_t rwo = __builtin_amdgcn_make_buffer_rsrc((void*)wo, 0, ((NH * DH - 1) * DM + 64) * 2, 0x00020000);
  const unsigned voffWo = (8u * w + (lane >> 3)) * 1024u + (((lane & 7u) ^ (((lane >> 4) & 1u) << 2)) << 4);
  const unsigned hh = lane >> 5, q4 = (lane >> 2) & 3u, p4 = lane & 3u, dhalf = (lane >> 4) & 1u;
  const unsigned coloff = 16u * (p4 & 1u) + 8u * dhalf + 4u * (p4 >> 1);
  const unsigned lbA = ldsb + (4u * hh + q4) * 128u + (((q4 >> 1) & 1u) << 6) + 2u * coloff;
  const char* obase = (const char*)ows + ((size_t)pair * NH * 4u + w) * 8192u;
  const unsigned vo = lane * 16u, vo2 = lane * 16u + 4096u;

  auto issue = [&](unsigned hd) {
    const unsigned m0b = ldsb + (hd & 3u) * SLOT + w * 1024u;
#pragma unroll
    for (unsigned i = 0; i < 4; ++i) dma16(m0b + i * 4096u, voffWo, rwo, hd * 131072u + i * 32768u);
  };
  bf16x8 B0[8], B1[8], B2[8], B3[8];
  f32x16 acc0 = {}, acc1 = {};
  issue(0); LDO8(B0, obase);
  issue(1); LDO8(B1, obase + 32768);
  issue(2); LDO8(B2, obase + 2 * 32768);

#define STEP(hd, SC, SN, WN)                                                              \
  do {                                                                                    \
    WAITO(WN, SC);                                                                        \
    BARRIER();                                                                            \
    if ((hd) + 3u < 32u) { issue((hd) + 3u); LDO8(SN, obase + (size_t)((hd) + 3u) * 32768u); } \
    const unsigned a0 = lbA + ((hd) & 3u) * SLOT;                                         \
    _Pragma("unroll") for (unsigned i = 0; i < 8; ++i) {                                  \
      bf16x8 f0 = lds_tr2(a0 + 2048u * i, a0 + 2048u * i + 1024u);                        \
      bf16x8 f1 = lds_tr2((a0 ^ 64u) + 2048u * i, (a0 ^ 64u) + 2048u * i + 1024u);        \
      acc0 = MFMA(f0, SC[i], acc0);                                                       \
      acc1 = MFMA(f1, SC[i], acc1);                                                       \
    }                                                                                     \
  } while (0)

  for (unsigned hd = 0; hd < 28; hd += 4) {
    STEP(hd, B0, B3, 24);
    STEP(hd + 1, B1, B0, 24);
    STEP(hd + 2, B2, B1, 24);
    STEP(hd + 3, B3, B2, 24);
  }
  STEP(28u, B0, B3, 24);
  STEP(29u, B1, B0, 24);
  STEP(30u, B2, B1, 12);
  STEP(31u, B3, B2, 0);
#undef STEP
  const unsigned r32 = lane & 31u;
  __bf16* orow = out + ((size_t)b * SQ + 32u * w + r32) * 1024u + br * 512u + nt * 64u + 16u * hh;
  *(bf16x8*)(orow) = pack8n(acc0, 0);
  *(bf16x8*)(orow + 8) = pack8n(acc0, 8);
  *(bf16x8*)(orow + 32) = pack8n(acc1, 0);
  *(bf16x8*)(orow + 40) = pack8n(acc1, 8);
}

extern "C" void kernel_launch(void* const* d_in, const int* in_sizes, int n_in, void* d_out, int out_size, void* d_ws,
                              size_t ws_size, hipStream_t stream) {
  const __bf16* xA = (const __bf16*)d_in[0];
  const __bf16* kA = (const __bf16*)d_in[1];
  const __bf16* vA = (const __bf16*)d_in[2];
  const __bf16* wqA = (const __bf16*)d_in[3];
  const __bf16* woA = (const __bf16*)d_in[4];
  const __bf16* xB = (const __bf16*)d_in[5];
  const __bf16* kB = (const __bf16*)d_in[6];
  const __bf16* vB = (const __bf16*)d_in[7];
  const __bf16* wqB = (const __bf16*)d_in[8];
  const __bf16* woB = (const __bf16*)d_in[9];
  __bf16* ows = (__bf16*)d_ws;
  hipFuncSetAttribute((const void*)attn_kernel, hipFuncAttributeMaxDynamicSharedMemorySize, NSLOT * SLOT);
  hipFuncSetAttribute((const void*)oproj_kernel, hipFuncAttributeMaxDynamicSharedMemorySize, 4 * SLOT);
  hipLaunchKernelGGL(attn_kernel, dim3(1024), dim3(256), NSLOT * SLOT, stream, xA, kA, vA, wqA, xB, kB, vB, wqB, ows);
  hipLaunchKernelGGL(oproj_kernel, dim3(256), dim3(256), 4 * SLOT, stream, (const __bf16*)ows, woA, woB, (__bf16*)d_out);
}
```

```cpp
#include <hip/hip_runtime.h>
#include <hip/hip_bf16.h>
#include <stdint.h>

typedef __attribute__((ext_vector_type(8))) __bf16 bf16x8;
typedef __attribute__((ext_vector_type(4))) __bf16 bf16x4;
typedef __attribute__((ext_vector_type(2))) __bf16 bf16x2;
typedef __attribute__((ext_vector_type(16))) float f32x16;
typedef __attribute__((ext_vector_type(2))) float f32x2;
typedef __attribute__((ext_vector_type(4))) unsigned u32x4;
#define LAS __attribute__((address_space(3)))
#define DI __device__ __forceinline__
typedef __amdgpu_buffer_rsrc_t rsrc_t;

constexpr int NB = 16, SQ = 128, DM = 512, NH = 32, SK = 512, DH = 128;
constexpr unsigned SLOT = 16384, NSLOT = 5;

#define WAITV(n) asm volatile("s_waitcnt vmcnt(" #n ")" ::: "memory")
#define BARRIER() asm volatile("s_waitcnt lgkmcnt(0)\n\ts_barrier" ::: "memory")

DI void dma16(unsigned m0v, unsigned voff, rsrc_t r, unsigned soff) {
  asm volatile("s_mov_b32 m0, %0\n\ts_nop 0\n\tbuffer_load_dwordx4 %1, %2, %3 offen lds"
               :: "s"(m0v), "v"(voff), "s"(r), "s"(soff) : "memory");
}
DI void dma16nt(unsigned m0v, unsigned voff, rsrc_t r, unsigned soff) {
  asm volatile("s_mov_b32 m0, %0\n\ts_nop 0\n\tbuffer_load_dwordx4 %1, %2, %3 offen nt lds"
               :: "s"(m0v), "v"(voff), "s"(r), "s"(soff) : "memory");
}
DI unsigned cvtpk(float lo, float hi) {
  f32x2 v = {lo, hi};
  bf16x2 b = __builtin_convertvector(v, bf16x2);
  return __builtin_bit_cast(unsigned, b);
}
DI bf16x8 pack8(const f32x16& a, int base, float sc) {
  u32x4 w = {cvtpk(a[base + 0] * sc, a[base + 1] * sc), cvtpk(a[base + 2] * sc, a[base + 3] * sc),
             cvtpk(a[base + 4] * sc, a[base + 5] * sc), cvtpk(a[base + 6] * sc, a[base + 7] * sc)};
  return __builtin_bit_cast(bf16x8, w);
}
DI bf16x8 pack8n(const f32x16& a, int base) {
  u32x4 w = {cvtpk(a[base + 0], a[base + 1]), cvtpk(a[base + 2], a[base + 3]),
             cvtpk(a[base + 4], a[base + 5]), cvtpk(a[base + 6], a[base + 7])};
  return __builtin_bit_cast(bf16x8, w);
}
DI bf16x8 lds_b128(unsigned addr) { return *(LAS const bf16x8*)(addr); }
DI bf16x8 lds_tr2(unsigned addr0, unsigned addr1) {
  bf16x4 lo = __builtin_amdgcn_ds_read_tr16_b64_v4bf16((LAS bf16x4*)(addr0));
  bf16x4 hi = __builtin_amdgcn_ds_read_tr16_b64_v4bf16((LAS bf16x4*)(addr1));
  return __builtin_shufflevector(lo, hi, 0, 1, 2, 3, 4, 5, 6, 7);
}
DI float xhalf_max(float x) {
  auto r = __builtin_amdgcn_permlane32_swap(__float_as_uint(x), __float_as_uint(x), false, false);
  return fmaxf(__uint_as_float(r[0]), __uint_as_float(r[1]));
}
DI float xhalf_sum(float x) {
  auto r = __builtin_amdgcn_permlane32_swap(__float_as_uint(x), __float_as_uint(x), false, false);
  return __uint_as_float(r[0]) + __uint_as_float(r[1]);
}
#define MFMA(a, b, c) __builtin_amdgcn_mfma_f32_32x32x16_bf16((a), (b), (c), 0, 0, 0)

#define SB() __builtin_amdgcn_sched_barrier(0)
DI float max3f(float a, float b, float c) { float r; asm("v_max3_f32 %0, %1, %2, %3" : "=v"(r) : "v"(a), "v"(b), "v"(c)); return r; }
DI void waitv_dyn(unsigned y) {
  if (y >= 3u) {
    if (y == 3u) WAITV(12); else if (y == 4u) WAITV(16); else WAITV(20);
  } else {
    if (y == 2u) WAITV(8); else if (y == 1u) WAITV(4); else WAITV(0);
  }
}

__global__ __launch_bounds__(256, 2) void attn_kernel(
    const __bf16* __restrict__ xA, const __bf16* __restrict__ kA, const __bf16* __restrict__ vA, const __bf16* __restrict__ wqA,
    const __bf16* __restrict__ xB, const __bf16* __restrict__ kB, const __bf16* __restrict__ vB, const __bf16* __restrict__ wqB,
    __bf16* __restrict__ ows) {
  extern __shared__ __attribute__((aligned(16))) char smem[];
  const unsigned ldsb = (unsigned)(size_t)(LAS char*)smem;
  const unsigned tid = threadIdx.x, lane = tid & 63u;
  const unsigned w = __builtin_amdgcn_readfirstlane(tid >> 6);
  const unsigned bid = blockIdx.x;
  const unsigned typeB = (__builtin_amdgcn_s_getreg(6 | (11 << 11)) != 0) ? 1u : 0u;
  const unsigned xcd = bid & 7u, idx = bid >> 3;
  const unsigned br = xcd >> 2, h = (xcd & 3u) * 8u + (idx & 7u), b0 = idx >> 3;
  const rsrc_t rx = __builtin_amdgcn_make_buffer_rsrc((void*)(br ? xB : xA), 0, NB * SQ * DM * 2, 0x00020000);
  const rsrc_t rw = __builtin_amdgcn_make_buffer_rsrc((void*)((br ? wqB : wqA) + (size_t)h * DH), 0, ((DM - 1) * NH * DH + DH) * 2, 0x00020000);
  const rsrc_t rk = __builtin_amdgcn_make_buffer_rsrc((void*)(br ? kB : kA), 0, NB * NH * SK * DH * 2, 0x00020000);
  const rsrc_t rv = __builtin_amdgcn_make_buffer_rsrc((void*)(br ? vB : vA), 0, NB * NH * SK * DH * 2, 0x00020000);

  const unsigned l4 = lane >> 4, l15 = lane & 15u;
  const unsigned voffK = (4u * w + l4) * 256u + ((l15 ^ (4u * w + l4)) << 4);
  const unsigned voffV = (4u * w + l4) * 256u + ((l15 ^ (l4 << 2)) << 4);
  const unsigned voffW = (4u * w + l4) * 8192u + ((l15 ^ (l4 << 2)) << 4);
  const unsigned voffX = (8u * w + (lane >> 3)) * 1024u + (((lane & 7u) ^ ((4u * w + l4) & 7u)) << 4);
  const unsigned r32 = lane & 31u, hh = lane >> 5;
  const unsigned q4 = (lane >> 2) & 3u, p4 = lane & 3u, dhalf = (lane >> 4) & 1u;
  const unsigned lbK = ldsb + r32 * 256u + ((hh ^ (r32 & 15u)) << 4);
  const unsigned lbV = ldsb + (4u * hh + q4) * 256u + (q4 << 6) + 32u * dhalf + 8u * p4;
  const unsigned psw = ((p4 & 1u) << 1) | (p4 >> 1);
  const unsigned lbW = ldsb + (8u * hh + q4) * 256u + (q4 << 6) + 32u * dhalf + 8u * psw;
  const unsigned lbX = ldsb + (32u * w + r32) * 128u + ((hh ^ ((r32 >> 1) & 7u)) << 4);

  auto kindQ = [&](unsigned seg) -> bool { return typeB ? (seg < 2u) : ((seg & 1u) == 0u); };
  auto unitOf = [&](unsigned seg) -> unsigned { return typeB ? (seg & 1u) : (seg >> 1); };
  unsigned issued = 0, is = 0;
  auto issue1 = [&]() {
    const unsigned n = issued;
    if (n < 64u) {
      const unsigned seg = n >> 4, m = n & 15u, j = m >> 1;
      const unsigned b = b0 + 8u * unitOf(seg);
      const unsigned m0b = ldsb + is * SLOT + w * 1024u;
      if (kindQ(seg)) {
        if ((m & 1u) == 0) {
          const unsigned so = b * (SQ * DM * 2) + j * 128u;
#pragma unroll
          for (unsigned i = 0; i < 4; ++i) dma16(m0b + i * 4096u, voffX, rx, so + i * 32768u);
        } else {
          const unsigned so = j * 64u * 8192u;
#pragma unroll
          for (unsigned i = 0; i < 4; ++i) dma16(m0b + i * 4096u, voffW, rw, so + i * 16u * 8192u);
        }
      } else {
        const unsigned so = (b * NH + h) * (SK * DH * 2) + j * 16384u;
        if ((m & 1u) == 0) {
#pragma unroll
          for (unsigned i = 0; i < 4; ++i) dma16nt(m0b + i * 4096u, voffK, rk, so + i * 4096u);
        } else {
#pragma unroll
          for (unsigned i = 0; i < 4; ++i) dma16nt(m0b + i * 4096u, voffV, rv, so + i * 4096u);
        }
      }
      issued = n + 1u;
      is = is == NSLOT - 1 ? 0u : is + 1u;
    }
  };
  auto nxt = [](unsigned s) { return s == NSLOT - 1 ? 0u : s + 1u; };
  unsigned n = 0, cs = 0, stmark = 0;
  auto sync = [&](unsigned need) {
    if (need > issued) need = issued;
    waitv_dyn(issued - need + (need <= stmark ? 2u : 0u));
    BARRIER();
    while (issued < n + NSLOT && issued < 64u) issue1();
  };

  issue1(); issue1(); issue1(); issue1(); issue1();

  constexpr float C = 0.08838834764831845f * 1.4426950408889634f;
  constexpr float THRS = 8.0f / C;
  bf16x8 qf[8], qn[8];
  bf16x8 kf0, kf1, kf2, kf3;
  bool kpre = false;

  for (unsigned seg = 0; seg < 4; ++seg) {
    if (kindQ(seg)) {
      f32x16 acc[4] = {};
      for (unsigned j = 0; j < 8; ++j) {
        sync(n + 2u);
        const unsigned ax = lbX + cs * SLOT; cs = nxt(cs);
        const unsigned aw = lbW + cs * SLOT; cs = nxt(cs);
        n += 2u;
        bf16x8 xb[4], af[16];
        xb[0] = lds_b128(ax);
#pragma unroll
        for (unsigned i = 0; i < 4; ++i) { const unsigned a = (aw ^ (i << 6)); af[i] = lds_tr2(a, a + 1024u); }
        xb[1] = lds_b128(ax ^ 32u);
        SB();
#pragma unroll
        for (unsigned i = 0; i < 16; ++i) {
          if (i + 4 < 16) { const unsigned a = (aw ^ (((i + 4) & 3u) << 6)) + 4096u * ((i + 4) >> 2); af[i + 4] = lds_tr2(a, a + 1024u); }
          if (i == 2) xb[2] = lds_b128(ax ^ 64u);
          if (i == 6) xb[3] = lds_b128(ax ^ 96u);
          SB();
          acc[i & 3] = MFMA(af[i], xb[i >> 2], acc[i & 3]);
          SB();
        }
      }
      if (typeB && seg == 1u) {
#pragma unroll
        for (int db = 0; db < 4; ++db) { qn[2 * db] = pack8n(acc[db], 0); qn[2 * db + 1] = pack8n(acc[db], 8); }
      } else {
#pragma unroll
        for (int db = 0; db < 4; ++db) { qf[2 * db] = pack8n(acc[db], 0); qf[2 * db + 1] = pack8n(acc[db], 8); }
      }
      kpre = false;
    } else {
      if (typeB && seg == 3u) {
#pragma unroll
        for (int i = 0; i < 8; ++i) qf[i] = qn[i];
      }
      f32x16 o[4] = {};
      float m = 0.f, l = 0.f;
      for (unsigned t = 0; t < 8; ++t) {
        sync(n + 2u);
        const unsigned ak = lbK + cs * SLOT; cs = nxt(cs);
        const unsigned av = lbV + cs * SLOT;
        n += 1u;
        if (!kpre) { kf0 = lds_b128(ak); kf1 = lds_b128(ak + 8192u); kf2 = lds_b128(ak ^ 32u); kf3 = lds_b128((ak ^ 32u) + 8192u); }
        bf16x8 kf[16], vf[16];
        kf[0] = kf0; kf[1] = kf1; kf[2] = kf2; kf[3] = kf3;
        f32x16 s0 = {}, s1 = {};
        SB();
#pragma unroll
        for (unsigned i = 0; i < 16; ++i) {
          if (i + 4 < 16) kf[i + 4] = lds_b128((ak ^ (((i + 4) >> 1) << 5)) + 8192u * ((i + 4) & 1u));
          else { const unsigned a = av ^ ((i - 12) << 6); vf[i - 12] = lds_tr2(a, a + 2048u); }
          SB();
          if (i & 1) s1 = MFMA(kf[i], qf[i >> 1], s1); else s0 = MFMA(kf[i], qf[i >> 1], s0);
          SB();
        }
        const bool lastv = (t == 7u) && !(typeB && seg == 2u);
        sync(n + (lastv ? 1u : 2u));
        cs = nxt(cs);
        const unsigned akn = lbK + cs * SLOT;
        n += 1u;
        float tm0 = max3f(s0[0], s0[1], s0[2]), tm1 = max3f(s1[0], s1[1], s1[2]);
#pragma unroll
        for (int r = 3; r < 15; r += 2) { tm0 = max3f(tm0, s0[r], s0[r + 1]); tm1 = max3f(tm1, s1[r], s1[r + 1]); }
        float tmax = max3f(tm0, tm1, s0[15]);
        tmax = max3f(tmax, s1[15], s1[15]);
        {
          auto r = __builtin_amdgcn_permlane32_swap(__float_as_uint(tmax), __float_as_uint(tmax), false, false);
          tmax = max3f(__uint_as_float(r[0]), __uint_as_float(r[1]), __uint_as_float(r[1]));
        }
        if (t == 0) {
          m = tmax;
        } else if (__any(tmax - m > THRS)) {
          const float mn = fmaxf(m, tmax);
          const float alpha = __builtin_amdgcn_exp2f((m - mn) * C);
          m = mn; l *= alpha;
#pragma unroll
          for (int db = 0; db < 4; ++db)
#pragma unroll
            for (int r = 0; r < 16; ++r) o[db][r] *= alpha;
        }
        const float mc = -m * C;
        float ps0 = 0.f, ps1 = 0.f;
        u32x4 pw[4];
#define EXP2(S, R) S[R] = __builtin_amdgcn_exp2f(fmaf(S[R], C, mc))
#pragma unroll
        for (int e = 0; e < 4; ++e) {
          EXP2(s0, 2 * e); EXP2(s0, 2 * e + 1);
          pw[0][e] = cvtpk(s0[2 * e], s0[2 * e + 1]);
          ps0 += s0[2 * e]; ps1 += s0[2 * e + 1];
        }
        SB();
#pragma unroll
        for (unsigned i = 0; i < 16; ++i) {
          const unsigned ks = i >> 2, db = i & 3u;
          if (i + 4 < 16) { const unsigned a = (av ^ (((i + 4) & 3u) << 6)) + 4096u * ((i + 4) >> 2); vf[i + 4] = lds_tr2(a, a + 2048u); }
          else if (i == 12) kf0 = lds_b128(akn);
          else if (i == 13) kf1 = lds_b128(akn + 8192u);
          else if (i == 14) kf2 = lds_b128(akn ^ 32u);
          else kf3 = lds_b128((akn ^ 32u) + 8192u);
          SB();
          o[db] = MFMA(vf[i], __builtin_bit_cast(bf16x8, pw[ks]), o[db]);
          SB();
          if (ks < 3) {
            const int r = 8 * ((ks + 1) & 1) + 2 * db;
            if (ks + 1 < 2) {
              EXP2(s0, r); EXP2(s0, r + 1);
              pw[ks + 1][db] = cvtpk(s0[r], s0[r + 1]);
              ps0 += s0[r]; ps1 += s0[r + 1];
            } else {
              EXP2(s1, r); EXP2(s1, r + 1);
              pw[ks + 1][db] = cvtpk(s1[r], s1[r + 1]);
              ps0 += s1[r]; ps1 += s1[r + 1];
            }
          }
          SB();
        }
#undef EXP2
        l += ps0 + ps1;
        kpre = !lastv;
      }
      const float inv = 1.0f / xhalf_sum(l);
      const unsigned b = b0 + 8u * unitOf(seg);
      const unsigned u = (br * NB + b) * NH + h;
      bf16x8* op = (bf16x8*)ows + ((size_t)(u * 4u + w) * 8u) * 64u + lane;
#pragma unroll
      for (int db = 0; db < 4; ++db) {
        op[(2 * db) * 64] = pack8(o[db], 0, inv);
        op[(2 * db + 1) * 64] = pack8(o[db], 8, inv);
      }
      asm volatile("" ::: "memory");
      stmark = issued;
    }
  }
}

DI void ldo(bf16x8& d, unsigned voff, const void* base, const int imm) {
  asm volatile("global_load_dwordx4 %0, %1, %2 offset:%3" : "=v"(d) : "v"(voff), "s"(base), "i"(imm) : "memory");
}
#define LDO8(S, base)                                                                     \
  do {                                                                                    \
    ldo(S[0], vo, base, 0); ldo(S[1], vo, base, 1024); ldo(S[2], vo, base, 2048);         \
    ldo(S[3], vo, base, 3072); ldo(S[4], vo2, base, 0); ldo(S[5], vo2, base, 1024);       \
    ldo(S[6], vo2, base, 2048); ldo(S[7], vo2, base, 3072);                               \
  } while (0)
#define WAITO(n, S)                                                                       \
  asm volatile("s_waitcnt vmcnt(" #n ")"                                                  \
               : "+v"(S[0]), "+v"(S[1]), "+v"(S[2]), "+v"(S[3]), "+v"(S[4]), "+v"(S[5]),  \
                 "+v"(S[6]), "+v"(S[7])::"memory")

__global__ __launch_bounds__(256, 1) void oproj_kernel(const __bf16* __restrict__ ows, const __bf16* __restrict__ woA,
                                                       const __bf16* __restrict__ woB, __bf16* __restrict__ out) {
  extern __shared__ __attribute__((aligned(16))) char smem[];
  const unsigned ldsb = (unsigned)(size_t)(LAS char*)smem;
  const unsigned tid = threadIdx.x, lane = tid & 63u;
  const unsigned w = __builtin_amdgcn_readfirstlane(tid >> 6);
  const unsigned bid = blockIdx.x;
  const unsigned xcd = bid & 7u, idx = bid >> 3;
  const unsigned pair = xcd * 4u + (idx >> 3), nt = idx & 7u;
  const unsigned br = pair >> 4, b = pair & 15u;
  const __bf16* wo = (br ? woB : woA) + nt * 64u;
  const rsrc_t rwo = __builtin_amdgcn_make_buffer_rsrc((void*)wo, 0, ((NH * DH - 1) * DM + 64) * 2, 0x00020000);
  const unsigned voffWo = (8u * w + (lane >> 3)) * 1024u + (((lane & 7u) ^ (((lane >> 4) & 1u) << 2)) << 4);
  const unsigned hh = lane >> 5, q4 = (lane >> 2) & 3u, p4 = lane & 3u, dhalf = (lane >> 4) & 1u;
  const unsigned coloff = 16u * (p4 & 1u) + 8u * dhalf + 4u * (p4 >> 1);
  const unsigned lbA = ldsb + (4u * hh + q4) * 128u + (((q4 >> 1) & 1u) << 6) + 2u * coloff;
  const char* obase = (const char*)ows + ((size_t)pair * NH * 4u + w) * 8192u;
  const unsigned vo = lane * 16u, vo2 = lane * 16u + 4096u;

  auto issue = [&](unsigned hd) {
    const unsigned m0b = ldsb + (hd & 3u) * SLOT + w * 1024u;
#pragma unroll
    for (unsigned i = 0; i < 4; ++i) dma16(m0b + i * 4096u, voffWo, rwo, hd * 131072u + i * 32768u);
  };
  bf16x8 B0[8], B1[8], B2[8], B3[8];
  f32x16 acc0 = {}, acc1 = {};
  issue(0); LDO8(B0, obase);
  issue(1); LDO8(B1, obase + 32768);
  issue(2); LDO8(B2, obase + 2 * 32768);

#define STEP(hd, SC, SN, WN)                                                              \
  do {                                                                                    \
    WAITO(WN, SC);                                                                        \
    BARRIER();                                                                            \
    if ((hd) + 3u < 32u) { issue((hd) + 3u); LDO8(SN, obase + (size_t)((hd) + 3u) * 32768u); } \
    const unsigned a0 = lbA + ((hd) & 3u) * SLOT;                                         \
    _Pragma("unroll") for (unsigned i = 0; i < 8; ++i) {                                  \
      bf16x8 f0 = lds_tr2(a0 + 2048u * i, a0 + 2048u * i + 1024u);                        \
      bf16x8 f1 = lds_tr2((a0 ^ 64u) + 2048u * i, (a0 ^ 64u) + 2048u * i + 1024u);        \
      acc0 = MFMA(f0, SC[i], acc0);                                                       \
      acc1 = MFMA(f1, SC[i], acc1);                                                       \
    }                                                                                     \
  } while (0)

  for (unsigned hd = 0; hd < 28; hd += 4) {
    STEP(hd, B0, B3, 24);
    STEP(hd + 1, B1, B0, 24);
    STEP(hd + 2, B2, B1, 24);
    STEP(hd + 3, B3, B2, 24);
  }
  STEP(28u, B0, B3, 24);
  STEP(29u, B1, B0, 24);
  STEP(30u, B2, B1, 12);
  STEP(31u, B3, B2, 0);
#undef STEP
  const unsigned r32 = lane & 31u;
  __bf16* orow = out + ((size_t)b * SQ + 32u * w + r32) * 1024u + br * 512u + nt * 64u + 16u * hh;
  *(bf16x8*)(orow) = pack8n(acc0, 0);
  *(bf16x8*)(orow + 8) = pack8n(acc0, 8);
  *(bf16x8*)(orow + 32) = pack8n(acc1, 0);
  *(bf16x8*)(orow + 40) = pack8n(acc1, 8);
}

extern "C" void kernel_launch(void* const* d_in, const int* in_sizes, int n_in, void* d_out, int out_size, void* d_ws,
                              size_t ws_size, hipStream_t stream) {
  const __bf16* xA = (const __bf16*)d_in[0];
  const __bf16* kA = (const __bf16*)d_in[1];
  const __bf16* vA = (const __bf16*)d_in[2];
  const __bf16* wqA = (const __bf16*)d_in[3];
  const __bf16* woA = (const __bf16*)d_in[4];
  const __bf16* xB = (const __bf16*)d_in[5];
  const __bf16* kB = (const __bf16*)d_in[6];
  const __bf16* vB = (const __bf16*)d_in[7];
  const __bf16* wqB = (const __bf16*)d_in[8];
  const __bf16* woB = (const __bf16*)d_in[9];
  __bf16* ows = (__bf16*)d_ws;
  hipFuncSetAttribute((const void*)attn_kernel, hipFuncAttributeMaxDynamicSharedMemorySize, NSLOT * SLOT);
  hipFuncSetAttribute((const void*)oproj_kernel, hipFuncAttributeMaxDynamicSharedMemorySize, 4 * SLOT);
  hipLaunchKernelGGL(attn_kernel, dim3(512), dim3(256), NSLOT * SLOT, stream, xA, kA, vA, wqA, xB, kB, vB, wqB, ows);
  hipLaunchKernelGGL(oproj_kernel, dim3(256), dim3(256), 4 * SLOT, stream, (const __bf16*)ows, woA, woB, (__bf16*)d_out);
}
```

```cpp
#include <hip/hip_runtime.h>
#include <hip/hip_bf16.h>
#include <stdint.h>

typedef __attribute__((ext_vector_type(8))) __bf16 bf16x8;
typedef __attribute__((ext_vector_type(4))) __bf16 bf16x4;
typedef __attribute__((ext_vector_type(2))) __bf16 bf16x2;
typedef __attribute__((ext_vector_type(16))) float f32x16;
typedef __attribute__((ext_vector_type(2))) float f32x2;
typedef __attribute__((ext_vector_type(4))) unsigned u32x4;
#define LAS __attribute__((address_space(3)))
#define DI __device__ __forceinline__
#define AI __attribute__((always_inline))
typedef __amdgpu_buffer_rsrc_t rsrc_t;

constexpr int NB = 16, SQ = 128, DM = 512, NH = 32, SK = 512, DH = 128;
constexpr unsigned SLOT = 16384, NSLOT = 5;

#define WAITV(n) asm volatile("s_waitcnt vmcnt(" #n ")" ::: "memory")
#define BARRIER() asm volatile("s_waitcnt lgkmcnt(0)\n\ts_barrier" ::: "memory")

DI void dma16(unsigned m0v, unsigned voff, rsrc_t r, unsigned soff) {
  asm volatile("s_mov_b32 m0, %0\n\ts_nop 0\n\tbuffer_load_dwordx4 %1, %2, %3 offen lds"
               :: "s"(m0v), "v"(voff), "s"(r), "s"(soff) : "memory");
}
DI void dma16nt(unsigned m0v, unsigned voff, rsrc_t r, unsigned soff) {
  asm volatile("s_mov_b32 m0, %0\n\ts_nop 0\n\tbuffer_load_dwordx4 %1, %2, %3 offen nt lds"
               :: "s"(m0v), "v"(voff), "s"(r), "s"(soff) : "memory");
}
DI unsigned cvtpk(float lo, float hi) {
  f32x2 v = {lo, hi};
  bf16x2 b = __builtin_convertvector(v, bf16x2);
  return __builtin_bit_cast(unsigned, b);
}
DI bf16x8 pack8(const f32x16& a, int base, float sc) {
  u32x4 w = {cvtpk(a[base + 0] * sc, a[base + 1] * sc), cvtpk(a[base + 2] * sc, a[base + 3] * sc),
             cvtpk(a[base + 4] * sc, a[base + 5] * sc), cvtpk(a[base + 6] * sc, a[base + 7] * sc)};
  return __builtin_bit_cast(bf16x8, w);
}
DI bf16x8 pack8n(const f32x16& a, int base) {
  u32x4 w = {cvtpk(a[base + 0], a[base + 1]), cvtpk(a[base + 2], a[base + 3]),
             cvtpk(a[base + 4], a[base + 5]), cvtpk(a[base + 6], a[base + 7])};
  return __builtin_bit_cast(bf16x8, w);
}
DI bf16x8 lds_b128(unsigned addr) { return *(LAS const bf16x8*)(addr); }
DI bf16x8 lds_tr2(unsigned addr0, unsigned addr1) {
  bf16x4 lo = __builtin_amdgcn_ds_read_tr16_b64_v4bf16((LAS bf16x4*)(addr0));
  bf16x4 hi = __builtin_amdgcn_ds_read_tr16_b64_v4bf16((LAS bf16x4*)(addr1));
  return __builtin_shufflevector(lo, hi, 0, 1, 2, 3, 4, 5, 6, 7);
}
DI float xhalf_max(float x) {
  auto r = __builtin_amdgcn_permlane32_swap(__float_as_uint(x), __float_as_uint(x), false, false);
  return fmaxf(__uint_as_float(r[0]), __uint_as_float(r[1]));
}
DI float xhalf_sum(float x) {
  auto r = __builtin_amdgcn_permlane32_swap(__float_as_uint(x), __float_as_uint(x), false, false);
  return __uint_as_float(r[0]) + __uint_as_float(r[1]);
}
#define MFMA(a, b, c) __builtin_amdgcn_mfma_f32_32x32x16_bf16((a), (b), (c), 0, 0, 0)

#define SB() __builtin_amdgcn_sched_barrier(0)
DI float max3f(float a, float b, float c) { float r; asm("v_max3_f32 %0, %1, %2, %3" : "=v"(r) : "v"(a), "v"(b), "v"(c)); return r; }
template <int I, int NT> DI void dma1(unsigned m0v, unsigned v, rsrc_t r, unsigned soff) {
  if (NT) asm volatile("s_mov_b32 m0, %0\n\ts_nop 0\n\tbuffer_load_dwordx4 %1, %2, %3 offen offset:%4 nt lds"
                       :: "s"(m0v), "v"(v), "s"(r), "s"(soff), "i"(I * 1024) : "memory");
  else asm volatile("s_mov_b32 m0, %0\n\ts_nop 0\n\tbuffer_load_dwordx4 %1, %2, %3 offen offset:%4 lds"
                    :: "s"(m0v), "v"(v), "s"(r), "s"(soff), "i"(I * 1024) : "memory");
}
enum { TX = 0, TW = 1, TK = 2, TV = 3 };

struct Ctx {
  rsrc_t rx, rw, rk, rv;
  unsigned voffK, voffV, voffW, voffX, lbK, lbV, lbW, lbX, m0w;
  unsigned is, cs;
};
DI unsigned nxt(unsigned s) { return s == (NSLOT - 1) * SLOT ? 0u : s + SLOT; }
template <int T, int I> DI void piece(const Ctx& c, unsigned slot, unsigned so) {
  if (T == TX) dma1<I, 0>(c.m0w + slot, (c.voffX ^ ((I & 1) << 6)) + I * 7168u, c.rx, so);
  if (T == TW) dma1<I, 0>(c.m0w + slot, c.voffW + I * 31744u, c.rw, so);
  if (T == TK) dma1<I, 1>(c.m0w + slot, c.voffK ^ (I << 6), c.rk, so);
  if (T == TV) dma1<I, 1>(c.m0w + slot, c.voffV, c.rv, so);
}
template <int T> DI void piece_rt(const Ctx& c, int i, unsigned slot, unsigned so) {
  if (i == 0) piece<T, 0>(c, slot, so);
  if (i == 1) piece<T, 1>(c, slot, so);
  if (i == 2) piece<T, 2>(c, slot, so);
  if (i == 3) piece<T, 3>(c, slot, so);
}
template <int T> DI void tile_all(Ctx& c, unsigned so) {
  piece<T, 0>(c, c.is, so); piece<T, 1>(c, c.is, so); piece<T, 2>(c, c.is, so); piece<T, 3>(c, c.is, so);
  c.is = nxt(c.is);
}

constexpr float SMC = 0.08838834764831845f * 1.4426950408889634f;
constexpr float THRS = 8.0f / SMC;
#define EXP2(S, R) S[R] = __builtin_amdgcn_exp2f(fmaf(S[R], SMC, mc))
#define KFRAG(a, i) lds_b128(((a) ^ (((i) >> 1) << 5)) + 8192u * ((i) & 1u))
DI bf16x8 vfrag(unsigned av, unsigned i) { const unsigned a = (av ^ ((i & 3u) << 6)) + 4096u * (i >> 2); return lds_tr2(a, a + 2048u); }

template <int T0, int T1> DI void qphase(Ctx& c, f32x16 (&acc)[4], unsigned so0, unsigned so1) {
  const unsigned ax = c.lbX + c.cs; c.cs = nxt(c.cs);
  const unsigned aw = c.lbW + c.cs; c.cs = nxt(c.cs);
  unsigned s0 = 0, s1 = 0;
  if (T0 >= 0) { s0 = c.is; c.is = nxt(c.is); }
  if (T1 >= 0) { s1 = c.is; c.is = nxt(c.is); }
  bf16x8 xb[4], af[16];
  xb[0] = lds_b128(ax);
#pragma unroll
  for (unsigned i = 0; i < 4; ++i) { const unsigned a = (aw ^ (i << 6)); af[i] = lds_tr2(a, a + 1024u); }
  xb[1] = lds_b128(ax ^ 32u);
  SB();
#pragma unroll
  for (unsigned i = 0; i < 16; i += 2) {
    if (i + 4 < 16) {
      const unsigned a = (aw ^ (((i + 4) & 3u) << 6)) + 4096u * ((i + 4) >> 2); af[i + 4] = lds_tr2(a, a + 1024u);
      const unsigned a2 = (aw ^ (((i + 5) & 3u) << 6)) + 4096u * ((i + 5) >> 2); af[i + 5] = lds_tr2(a2, a2 + 1024u);
    }
    if (i == 2) xb[2] = lds_b128(ax ^ 64u);
    if (i == 6) xb[3] = lds_b128(ax ^ 96u);
    SB();
    acc[i & 3] = MFMA(af[i], xb[i >> 2], acc[i & 3]);
    SB();
    if (i < 8) { if (T0 >= 0) piece_rt<(T0 >= 0 ? T0 : 0)>(c, i >> 1, s0, so0); }
    else { if (T1 >= 0) piece_rt<(T1 >= 0 ? T1 : 0)>(c, (i - 8) >> 1, s1, so1); }
    SB();
    acc[(i + 1) & 3] = MFMA(af[i + 1], xb[i >> 2], acc[(i + 1) & 3]);
    SB();
  }
}

template <bool FIN, bool KPRE, int T0, int T1>
DI void aphase(Ctx& c, const bf16x8 (&qf)[8], f32x16& n0, f32x16& n1, f32x16& p0, f32x16& p1, u32x4 (&pw)[4],
               bf16x8 (&pre)[4], float mc, float& l, unsigned so0, unsigned so1) {
  const unsigned ak = c.lbK + c.cs; c.cs = nxt(c.cs);
  const unsigned av = c.lbV + c.cs;
  unsigned s0 = 0, s1 = 0;
  if (T0 >= 0) { s0 = c.is; c.is = nxt(c.is); }
  if (T1 >= 0) { s1 = c.is; c.is = nxt(c.is); }
  bf16x8 kf[16];
  if (KPRE) { kf[0] = pre[0]; kf[1] = pre[1]; kf[2] = pre[2]; kf[3] = pre[3]; }
  else { kf[0] = KFRAG(ak, 0); kf[1] = KFRAG(ak, 1); kf[2] = KFRAG(ak, 2); kf[3] = KFRAG(ak, 3); }
  float ps0 = 0.f, ps1 = 0.f;
  SB();
#pragma unroll
  for (unsigned i = 0; i < 16; i += 2) {
    if (i + 4 < 16) { kf[i + 4] = KFRAG(ak, i + 4); kf[i + 5] = KFRAG(ak, i + 5); }
    else if (FIN) { pre[i - 12] = vfrag(av, i - 12); pre[i - 11] = vfrag(av, i - 11); }
    SB();
    if (i == 0) { f32x16 z = {}; n0 = MFMA(kf[i], qf[i >> 1], z); } else n0 = MFMA(kf[i], qf[i >> 1], n0);
    SB();
    if (FIN) {
      EXP2(p1, i); EXP2(p1, i + 1);
      pw[i >> 3][(i >> 1) & 3] = cvtpk(p0[i], p0[i + 1]);
    }
    if (i < 8) { if (T0 >= 0) piece_rt<(T0 >= 0 ? T0 : 0)>(c, i >> 1, s0, so0); }
    else { if (T1 >= 0) piece_rt<(T1 >= 0 ? T1 : 0)>(c, (i - 8) >> 1, s1, so1); }
    SB();
    if (i == 0) { f32x16 z = {}; n1 = MFMA(kf[i + 1], qf[i >> 1], z); } else n1 = MFMA(kf[i + 1], qf[i >> 1], n1);
    SB();
    if (FIN) {
      pw[2 + (i >> 3)][(i >> 1) & 3] = cvtpk(p1[i], p1[i + 1]);
      ps0 += p1[i]; ps1 += p1[i + 1];
    }
    SB();
  }
  if (FIN) l += ps0 + ps1;
}

template <bool FIRST> DI void sm_max(const f32x16& s0, const f32x16& s1, float& m, float& l, float& mc, float& alpha, bool& resc) {
  float tm0 = max3f(s0[0], s0[1], s0[2]), tm1 = max3f(s1[0], s1[1], s1[2]);
#pragma unroll
  for (int r = 3; r < 15; r += 2) { tm0 = max3f(tm0, s0[r], s0[r + 1]); tm1 = max3f(tm1, s1[r], s1[r + 1]); }
  float tmax = max3f(tm0, tm1, s0[15]);
  tmax = max3f(tmax, s1[15], s1[15]);
  auto r = __builtin_amdgcn_permlane32_swap(__float_as_uint(tmax), __float_as_uint(tmax), false, false);
  tmax = max3f(__uint_as_float(r[0]), __uint_as_float(r[1]), __uint_as_float(r[1]));
  resc = false;
  if (FIRST) {
    m = tmax; mc = -m * SMC;
  } else if (__any(tmax - m > THRS)) {
    const float mn = fmaxf(m, tmax);
    alpha = __builtin_amdgcn_exp2f((m - mn) * SMC);
    m = mn; l *= alpha; mc = -m * SMC; resc = true;
  }
}

template <bool LAST, bool KNEXT, bool VNEXT, int T0>
DI void bphase(Ctx& c, f32x16 (&o)[4], f32x16& s0, f32x16& s1, u32x4 (&pw)[4], bf16x8 (&pre)[4], float& m, float& l, float& mc,
               unsigned so0) {
  const unsigned av = c.lbV + c.cs; c.cs = nxt(c.cs);
  const unsigned an = (KNEXT ? c.lbK : c.lbV) + c.cs;
  unsigned sl0 = 0;
  if (T0 >= 0) { sl0 = c.is; c.is = nxt(c.is); }
  bf16x8 vf[16];
  vf[0] = pre[0]; vf[1] = pre[1]; vf[2] = pre[2]; vf[3] = pre[3];
  float ps0 = 0.f, ps1 = 0.f, alpha = 1.f;
  bool resc = false;
  if (LAST) {
#pragma unroll
    for (int e = 0; e < 8; ++e) pw[e >> 2][e & 3] = cvtpk(s0[2 * e], s0[2 * e + 1]);
  }
  SB();
#pragma unroll
  for (unsigned i = 0; i < 16; i += 2) {
    const unsigned ks = i >> 2, db = i & 3u;
    if (i + 4 < 16) { vf[i + 4] = vfrag(av, i + 4); vf[i + 5] = vfrag(av, i + 5); }
    else if (KNEXT) { pre[i - 12] = KFRAG(an, i - 12); pre[i - 11] = KFRAG(an, i - 11); }
    else if (VNEXT) { pre[i - 12] = vfrag(an, i - 12); pre[i - 11] = vfrag(an, i - 11); }
    SB();
    o[db] = MFMA(vf[i], __builtin_bit_cast(bf16x8, pw[ks]), o[db]);
    SB();
    if (i < 8 && T0 >= 0) piece_rt<(T0 >= 0 ? T0 : 0)>(c, i >> 1, sl0, so0);
    if (LAST) {
      if (i < 8) {
        EXP2(s1, 2 * i); EXP2(s1, 2 * i + 1);
        pw[2 + (i >> 2)][i & 3] = cvtpk(s1[2 * i], s1[2 * i + 1]);
        ps0 += s1[2 * i]; ps1 += s1[2 * i + 1];
      }
    } else {
      if (i == 2) sm_max<false>(s0, s1, m, l, mc, alpha, resc);
      if (i >= 4) { const int r = (i - 4) * 4 / 3; (void)r; }
      if (i >= 6) { EXP2(s0, (i - 6) / 2 * 3); EXP2(s0, (i - 6) / 2 * 3 + 1); ps0 += s0[(i - 6) / 2 * 3]; ps1 += s0[(i - 6) / 2 * 3 + 1]; }
    }
    SB();
    o[db + 1] = MFMA(vf[i + 1], __builtin_bit_cast(bf16x8, pw[ks]), o[db + 1]);
    SB();
    if (LAST) {
      if (i < 8) {
        EXP2(s1, 2 * i + 2); EXP2(s1, 2 * i + 3);
        pw[2 + (i >> 2)][(i & 3) + 1] = cvtpk(s1[2 * i + 2], s1[2 * i + 3]);
        ps0 += s1[2 * i + 2]; ps1 += s1[2 * i + 3];
      }
    } else {
      if (i >= 6) { EXP2(s0, (i - 6) / 2 * 3 + 2); ps0 += s0[(i - 6) / 2 * 3 + 2]; }
      if (i == 4) { EXP2(s0, 15); ps1 += s0[15]; }
    }
    SB();
  }
  l += ps0 + ps1;
  if (!LAST) {
    if (resc) {
#pragma unroll
      for (int db = 0; db < 4; ++db)
#pragma unroll
        for (int r = 0; r < 16; ++r) o[db][r] *= alpha;
    }
  }
}

__global__ __launch_bounds__(256, 2) void attn_kernel(
    const __bf16* __restrict__ xA, const __bf16* __restrict__ kA, const __bf16* __restrict__ vA, const __bf16* __restrict__ wqA,
    const __bf16* __restrict__ xB, const __bf16* __restrict__ kB, const __bf16* __restrict__ vB, const __bf16* __restrict__ wqB,
    __bf16* __restrict__ ows) {
  extern __shared__ __attribute__((aligned(16))) char smem[];
  const unsigned ldsb = (unsigned)(size_t)(LAS char*)smem;
  const unsigned tid = threadIdx.x, lane = tid & 63u;
  const unsigned w = __builtin_amdgcn_readfirstlane(tid >> 6);
  const unsigned bid = blockIdx.x;
  const unsigned typeB = (__builtin_amdgcn_s_getreg(6 | (11 << 11)) != 0) ? 1u : 0u;
  const unsigned xcd = bid & 7u, idx = bid >> 3;
  const unsigned br = xcd >> 2, h = (xcd & 3u) * 8u + (idx & 7u), b0 = idx >> 3;
  Ctx c;
  c.rx = __builtin_amdgcn_make_buffer_rsrc((void*)(br ? xB : xA), 0, NB * SQ * DM * 2, 0x00020000);
  c.rw = __builtin_amdgcn_make_buffer_rsrc((void*)((br ? wqB : wqA) + (size_t)h * DH), 0, ((DM - 1) * NH * DH + DH) * 2, 0x00020000);
  c.rk = __builtin_amdgcn_make_buffer_rsrc((void*)(br ? kB : kA), 0, NB * NH * SK * DH * 2, 0x00020000);
  c.rv = __builtin_amdgcn_make_buffer_rsrc((void*)(br ? vB : vA), 0, NB * NH * SK * DH * 2, 0x00020000);
  const unsigned l4 = lane >> 4, l15 = lane & 15u;
  c.voffK = (16u * w + l4) * 256u + ((l15 ^ l4) << 4);
  c.voffV = (16u * w + l4) * 256u + ((l15 ^ (l4 << 2)) << 4);
  c.voffW = (16u * w + l4) * 8192u + ((l15 ^ (l4 << 2)) << 4);
  c.voffX = (32u * w + (lane >> 3)) * 1024u + (((lane & 7u) ^ l4) << 4);
  const unsigned r32 = lane & 31u, hh = lane >> 5;
  const unsigned q4 = (lane >> 2) & 3u, p4 = lane & 3u, dhalf = (lane >> 4) & 1u;
  c.lbK = ldsb + r32 * 256u + ((hh ^ (r32 & 15u)) << 4);
  c.lbV = ldsb + (4u * hh + q4) * 256u + (q4 << 6) + 32u * dhalf + 8u * p4;
  const unsigned psw = ((p4 & 1u) << 1) | (p4 >> 1);
  c.lbW = ldsb + (8u * hh + q4) * 256u + (q4 << 6) + 32u * dhalf + 8u * psw;
  c.lbX = ldsb + (32u * w + r32) * 128u + ((hh ^ ((r32 >> 1) & 7u)) << 4);
  c.m0w = ldsb + w * 4096u;
  c.is = 0; c.cs = 0;
  constexpr unsigned WSTEP = 64u * 8192u, OOB = 0x7f000000u;

  tile_all<TX>(c, b0 * (SQ * DM * 2)); tile_all<TW>(c, 0); tile_all<TX>(c, b0 * (SQ * DM * 2) + 128u); tile_all<TW>(c, WSTEP);
  if (!typeB) __builtin_amdgcn_s_setprio(2);

  for (unsigned unit = 0; unit < 2; ++unit) {
    const unsigned b = b0 + 8u * unit;
    const unsigned xso = b * (SQ * DM * 2);
    const unsigned kv = (b * NH + h) * (SK * DH * 2);
    const unsigned xnx = unit == 0 ? xso + 8u * (SQ * DM * 2) : OOB;
    const unsigned wnx = unit == 0 ? 0u : OOB;
    bf16x8 qf[8];
    {
      f32x16 acc[4] = {};
      if (unit == 0) WAITV(8); else WAITV(16);
      BARRIER();
      qphase<-1, TX>(c, acc, 0, xso + 256u);
      if (unit == 0) WAITV(4); else WAITV(12);
      BARRIER();
      qphase<TW, TX>(c, acc, 2u * WSTEP, xso + 384u);
      for (unsigned j = 2; j < 6; ++j) {
        WAITV(4); BARRIER();
        qphase<TW, TX>(c, acc, (j + 1u) * WSTEP, xso + (j + 2u) * 128u);
      }
      WAITV(4); BARRIER();
      qphase<TW, TK>(c, acc, 7u * WSTEP, kv);
      WAITV(4); BARRIER();
      qphase<TK, TV>(c, acc, kv + 16384u, kv);
#pragma unroll
      for (int db = 0; db < 4; ++db) { qf[2 * db] = pack8n(acc[db], 0); qf[2 * db + 1] = pack8n(acc[db], 8); }
    }
    __builtin_amdgcn_s_setprio(0);
    f32x16 o[4] = {};
    f32x16 sA0, sA1, sB0, sB1;
    u32x4 pw[4];
    bf16x8 pre[4];
    float m, l = 0.f, mc;
    WAITV(8); BARRIER();
    aphase<false, false, TK, TV>(c, qf, sA0, sA1, sB0, sB1, pw, pre, 0.f, l, kv + 2u * 16384u, kv + 16384u);
    {
      float alpha; bool resc;
      sm_max<true>(sA0, sA1, m, l, mc, alpha, resc);
      float ps0 = 0.f, ps1 = 0.f;
#pragma unroll
      for (int r = 0; r < 16; r += 2) { EXP2(sA0, r); EXP2(sA0, r + 1); ps0 += sA0[r]; ps1 += sA0[r + 1]; }
      l += ps0 + ps1;
    }
    WAITV(8); BARRIER();
    aphase<true, false, TK, -1>(c, qf, sB0, sB1, sA0, sA1, pw, pre, mc, l, kv + 3u * 16384u, 0);
    WAITV(8); BARRIER();
    bphase<false, true, false, TV>(c, o, sB0, sB1, pw, pre, m, l, mc, kv + 2u * 16384u);
    for (unsigned t = 2; t < 6; t += 2) {
      WAITV(8); BARRIER();
      aphase<true, true, TK, -1>(c, qf, sA0, sA1, sB0, sB1, pw, pre, mc, l, kv + (t + 2u) * 16384u, 0);
      WAITV(8); BARRIER();
      bphase<false, true, false, TV>(c, o, sA0, sA1, pw, pre, m, l, mc, kv + (t + 1u) * 16384u);
      WAITV(8); BARRIER();
      aphase<true, true, TK, -1>(c, qf, sB0, sB1, sA0, sA1, pw, pre, mc, l, kv + (t + 3u) * 16384u, 0);
      WAITV(8); BARRIER();
      bphase<false, true, false, TV>(c, o, sB0, sB1, pw, pre, m, l, mc, kv + (t + 2u) * 16384u);
    }
    WAITV(8); BARRIER();
    aphase<true, true, TV, -1>(c, qf, sA0, sA1, sB0, sB1, pw, pre, mc, l, kv + 7u * 16384u, 0);
    WAITV(8); BARRIER();
    bphase<false, true, false, TX>(c, o, sA0, sA1, pw, pre, m, l, mc, xnx);
    WAITV(8); BARRIER();
    aphase<true, true, TW, -1>(c, qf, sB0, sB1, sA0, sA1, pw, pre, mc, l, wnx, 0);
    WAITV(8); BARRIER();
    bphase<false, false, true, TX>(c, o, sB0, sB1, pw, pre, m, l, mc, xnx + 128u);
    WAITV(12); BARRIER();
    bphase<true, false, false, TW>(c, o, sB0, sB1, pw, pre, m, l, mc, wnx + WSTEP);
    const float inv = 1.0f / xhalf_sum(l);
    const unsigned u = (br * NB + b) * NH + h;
    bf16x8* op = (bf16x8*)ows + ((size_t)(u * 4u + w) * 8u) * 64u + lane;
#pragma unroll
    for (int db = 0; db < 4; ++db) {
      op[(2 * db) * 64] = pack8(o[db], 0, inv);
      op[(2 * db + 1) * 64] = pack8(o[db], 8, inv);
    }
    asm volatile("" ::: "memory");
  }
}

DI void ldo(bf16x8& d, unsigned voff, const void* base, const int imm) {
  asm volatile("global_load_dwordx4 %0, %1, %2 offset:%3" : "=v"(d) : "v"(voff), "s"(base), "i"(imm) : "memory");
}
#define LDO8(S, base)                                                                     \
  do {                                                                                    \
    ldo(S[0], vo, base, 0); ldo(S[1], vo, base, 1024); ldo(S[2], vo, base, 2048);         \
    ldo(S[3], vo, base, 3072); ldo(S[4], vo2, base, 0); ldo(S[5], vo2, base, 1024);       \
    ldo(S[6], vo2, base, 2048); ldo(S[7], vo2, base, 3072);                               \
  } while (0)
#define WAITO(n, S)                                                                       \
  asm volatile("s_waitcnt vmcnt(" #n ")"                                                  \
               : "+v"(S[0]), "+v"(S[1]), "+v"(S[2]), "+v"(S[3]), "+v"(S[4]), "+v"(S[5]),  \
                 "+v"(S[6]), "+v"(S[7])::"memory")

__global__ __launch_bounds__(256, 1) void oproj_kernel(const __bf16* __restrict__ ows, const __bf16* __restrict__ woA,
                                                       const __bf16* __restrict__ woB, __bf16* __restrict__ out) {
  extern __shared__ __attribute__((aligned(16))) char smem[];
  const unsigned ldsb = (unsigned)(size_t)(LAS char*)smem;
  const unsigned tid = threadIdx.x, lane = tid & 63u;
  const unsigned w = __builtin_amdgcn_readfirstlane(tid >> 6);
  const unsigned bid = blockIdx.x;
  const unsigned xcd = bid & 7u, idx = bid >> 3;
  const unsigned pair = xcd * 4u + (idx >> 3), nt = idx & 7u;
  const unsigned br = pair >> 4, b = pair & 15u;
  const __bf16* wo = (br ? woB : woA) + nt * 64u;
  const rsrc_t rwo = __builtin_amdgcn_make_buffer_rsrc((void*)wo, 0, ((NH * DH - 1) * DM + 64) * 2, 0x00020000);
  const unsigned voffWo = (8u * w + (lane >> 3)) * 1024u + (((lane & 7u) ^ (((lane >> 4) & 1u) << 2)) << 4);
  const unsigned hh = lane >> 5, q4 = (lane >> 2) & 3u, p4 = lane & 3u, dhalf = (lane >> 4) & 1u;
  const unsigned coloff = 16u * (p4 & 1u) + 8u * dhalf + 4u * (p4 >> 1);
  const unsigned lbA = ldsb + (4u * hh + q4) * 128u + (((q4 >> 1) & 1u) << 6) + 2u * coloff;
  const char* obase = (const char*)ows + ((size_t)pair * NH * 4u + w) * 8192u;
  const unsigned vo = lane * 16u, vo2 = lane * 16u + 4096u;

  auto issue = [&](unsigned hd) {
    const unsigned m0b = ldsb + (hd & 3u) * SLOT + w * 1024u;
#pragma unroll
    for (unsigned i = 0; i < 4; ++i) dma16(m0b + i * 4096u, voffWo, rwo, hd * 131072u + i * 32768u);
  };
  bf16x8 B0[8], B1[8], B2[8], B3[8];
  f32x16 acc0 = {}, acc1 = {};
  issue(0); LDO8(B0, obase);
  issue(1); LDO8(B1, obase + 32768);
  issue(2); LDO8(B2, obase + 2 * 32768);

#define STEP(hd, SC, SN, WN)                                                              \
  do {                                                                                    \
    WAITO(WN, SC);                                                                        \
    BARRIER();                                                                            \
    if ((hd) + 3u < 32u) { issue((hd) + 3u); LDO8(SN, obase + (size_t)((hd) + 3u) * 32768u); } \
    const unsigned a0 = lbA + ((hd) & 3u) * SLOT;                                         \
    _Pragma("unroll") for (unsigned i = 0; i < 8; ++i) {                                  \
      bf16x8 f0 = lds_tr2(a0 + 2048u * i, a0 + 2048u * i + 1024u);                        \
      bf16x8 f1 = lds_tr2((a0 ^ 64u) + 2048u * i, (a0 ^ 64u) + 2048u * i + 1024u);        \
      acc0 = MFMA(f0, SC[i], acc0);                                                       \
      acc1 = MFMA(f1, SC[i], acc1);                                                       \
    }                                                                                     \
  } while (0)

  for (unsigned hd = 0; hd < 28; hd += 4) {
    STEP(hd, B0, B3, 24);
    STEP(hd + 1, B1, B0, 24);
    STEP(hd + 2, B2, B1, 24);
    STEP(hd + 3, B3, B2, 24);
  }
  STEP(28u, B0, B3, 24);
  STEP(29u, B1, B0, 24);
  STEP(30u, B2, B1, 12);
  STEP(31u, B3, B2, 0);
#undef STEP
  const unsigned r32 = lane & 31u;
  __bf16* orow = out + ((size_t)b * SQ + 32u * w + r32) * 1024u + br * 512u + nt * 64u + 16u * hh;
  *(bf16x8*)(orow) = pack8n(acc0, 0);
  *(bf16x8*)(orow + 8) = pack8n(acc0, 8);
  *(bf16x8*)(orow + 32) = pack8n(acc1, 0);
  *(bf16x8*)(orow + 40) = pack8n(acc1, 8);
}

extern "C" void kernel_launch(void* const* d_in, const int* in_sizes, int n_in, void* d_out, int out_size, void* d_ws,
                              size_t ws_size, hipStream_t stream) {
  const __bf16* xA = (const __bf16*)d_in[0];
  const __bf16* kA = (const __bf16*)d_in[1];
  const __bf16* vA = (const __bf16*)d_in[2];
  const __bf16* wqA = (const __bf16*)d_in[3];
  const __bf16* woA = (const __bf16*)d_in[4];
  const __bf16* xB = (const __bf16*)d_in[5];
  const __bf16* kB = (const __bf16*)d_in[6];
  const __bf16* vB = (const __bf16*)d_in[7];
  const __bf16* wqB = (const __bf16*)d_in[8];
  const __bf16* woB = (const __bf16*)d_in[9];
  __bf16* ows = (__bf16*)d_ws;
  hipFuncSetAttribute((const void*)attn_kernel, hipFuncAttributeMaxDynamicSharedMemorySize, NSLOT * SLOT);
  hipFuncSetAttribute((const void*)oproj_kernel, hipFuncAttributeMaxDynamicSharedMemorySize, 4 * SLOT);
  hipLaunchKernelGGL(attn_kernel, dim3(512), dim3(256), NSLOT * SLOT, stream, xA, kA, vA, wqA, xB, kB, vB, wqB, ows);
  hipLaunchKernelGGL(oproj_kernel, dim3(256), dim3(256), 4 * SLOT, stream, (const __bf16*)ows, woA, woB, (__bf16*)d_out);
}
```

```cpp
#include <hip/hip_runtime.h>
#include <hip/hip_bf16.h>
#include <stdint.h>

typedef __attribute__((ext_vector_type(8))) __bf16 bf16x8;
typedef __attribute__((ext_vector_type(4))) __bf16 bf16x4;
typedef __attribute__((ext_vector_type(2))) __bf16 bf16x2;
typedef __attribute__((ext_vector_type(16))) float f32x16;
typedef __attribute__((ext_vector_type(2))) float f32x2;
typedef __attribute__((ext_vector_type(4))) unsigned u32x4;
#define LAS __attribute__((address_space(3)))
#define DI __device__ __forceinline__
#define AI __attribute__((always_inline))
typedef __amdgpu_buffer_rsrc_t rsrc_t;

constexpr int NB = 16, SQ = 128, DM = 512, NH = 32, SK = 512, DH = 128;
constexpr unsigned SLOT = 16384, NSLOT = 5;

#define WAITV(n) asm volatile("s_waitcnt vmcnt(" #n ")" ::: "memory")
#define BARRIER() asm volatile("s_waitcnt lgkmcnt(0)\n\ts_barrier" ::: "memory")

DI void dma16(unsigned m0v, unsigned voff, rsrc_t r, unsigned soff) {
  asm volatile("s_mov_b32 m0, %0\n\ts_nop 0\n\tbuffer_load_dwordx4 %1, %2, %3 offen lds"
               :: "s"(m0v), "v"(voff), "s"(r), "s"(soff) : "memory");
}
DI void dma16nt(unsigned m0v, unsigned voff, rsrc_t r, unsigned soff) {
  asm volatile("s_mov_b32 m0, %0\n\ts_nop 0\n\tbuffer_load_dwordx4 %1, %2, %3 offen nt lds"
               :: "s"(m0v), "v"(voff), "s"(r), "s"(soff) : "memory");
}
DI unsigned cvtpk(float lo, float hi) {
  f32x2 v = {lo, hi};
  bf16x2 b = __builtin_convertvector(v, bf16x2);
  return __builtin_bit_cast(unsigned, b);
}
DI bf16x8 pack8(const f32x16& a, int base, float sc) {
  u32x4 w = {cvtpk(a[base + 0] * sc, a[base + 1] * sc), cvtpk(a[base + 2] * sc, a[base + 3] * sc),
             cvtpk(a[base + 4] * sc, a[base + 5] * sc), cvtpk(a[base + 6] * sc, a[base + 7] * sc)};
  return __builtin_bit_cast(bf16x8, w);
}
DI bf16x8 pack8n(const f32x16& a, int base) {
  u32x4 w = {cvtpk(a[base + 0], a[base + 1]), cvtpk(a[base + 2], a[base + 3]),
             cvtpk(a[base + 4], a[base + 5]), cvtpk(a[base + 6], a[base + 7])};
  return __builtin_bit_cast(bf16x8, w);
}
DI bf16x8 lds_b128(unsigned addr) { return *(LAS const bf16x8*)(addr); }
DI bf16x8 lds_tr2(unsigned addr0, unsigned addr1) {
  bf16x4 lo = __builtin_amdgcn_ds_read_tr16_b64_v4bf16((LAS bf16x4*)(addr0));
  bf16x4 hi = __builtin_amdgcn_ds_read_tr16_b64_v4bf16((LAS bf16x4*)(addr1));
  return __builtin_shufflevector(lo, hi, 0, 1, 2, 3, 4, 5, 6, 7);
}
DI float xhalf_max(float x) {
  auto r = __builtin_amdgcn_permlane32_swap(__float_as_uint(x), __float_as_uint(x), false, false);
  return fmaxf(__uint_as_float(r[0]), __uint_as_float(r[1]));
}
DI float xhalf_sum(float x) {
  auto r = __builtin_amdgcn_permlane32_swap(__float_as_uint(x), __float_as_uint(x), false, false);
  return __uint_as_float(r[0]) + __uint_as_float(r[1]);
}
#define MFMA(a, b, c) __builtin_amdgcn_mfma_f32_32x32x16_bf16((a), (b), (c), 0, 0, 0)

#define SB() __builtin_amdgcn_sched_barrier(0)
DI float max3f(float a, float b, float c) { float r; asm("v_max3_f32 %0, %1, %2, %3" : "=v"(r) : "v"(a), "v"(b), "v"(c)); return r; }
template <int I, int NT> DI void dma1(unsigned m0v, unsigned v, rsrc_t r, unsigned soff) {
  if (NT) asm volatile("s_mov_b32 m0, %0\n\ts_nop 0\n\tbuffer_load_dwordx4 %1, %2, %3 offen offset:%4 nt lds"
                       :: "s"(m0v), "v"(v), "s"(r), "s"(soff), "i"(I * 1024) : "memory");
  else asm volatile("s_mov_b32 m0, %0\n\ts_nop 0\n\tbuffer_load_dwordx4 %1, %2, %3 offen offset:%4 lds"
                    :: "s"(m0v), "v"(v), "s"(r), "s"(soff), "i"(I * 1024) : "memory");
}
enum { TX = 0, TW = 1, TK = 2, TV = 3 };

struct Ctx {
  rsrc_t rx, rw, rk, rv;
  unsigned voffK, voffV, voffW, voffX, lbK, lbV, lbW, lbX, m0w;
  unsigned is, cs;
};
DI unsigned nxt(unsigned s) { return s == (NSLOT - 1) * SLOT ? 0u : s + SLOT; }
template <int T, int I> DI void piece(const Ctx& c, unsigned slot, unsigned so) {
  if (T == TX) dma1<I, 0>(c.m0w + slot, (c.voffX ^ ((I & 1) << 6)) + I * 7168u, c.rx, so);
  if (T == TW) dma1<I, 0>(c.m0w + slot, c.voffW + I * 31744u, c.rw, so);
  if (T == TK) dma1<I, 1>(c.m0w + slot, c.voffK ^ (I << 6), c.rk, so);
  if (T == TV) dma1<I, 1>(c.m0w + slot, c.voffV, c.rv, so);
}
template <int T> DI void piece_rt(const Ctx& c, int i, unsigned slot, unsigned so) {
  if (i == 0) piece<T, 0>(c, slot, so);
  if (i == 1) piece<T, 1>(c, slot, so);
  if (i == 2) piece<T, 2>(c, slot, so);
  if (i == 3) piece<T, 3>(c, slot, so);
}
template <int T> DI void tile_all(Ctx& c, unsigned so) {
  piece<T, 0>(c, c.is, so); piece<T, 1>(c, c.is, so); piece<T, 2>(c, c.is, so); piece<T, 3>(c, c.is, so);
  c.is = nxt(c.is);
}

constexpr float SMC = 0.08838834764831845f * 1.4426950408889634f;
constexpr float THRS = 8.0f / SMC;
#define EXP2(S, R) S[R] = __builtin_amdgcn_exp2f(fmaf(S[R], SMC, mc))
#define KFRAG(a, i) lds_b128(((a) ^ (((i) >> 1) << 5)) + 8192u * ((i) & 1u))
DI bf16x8 vfrag(unsigned av, unsigned i) { const unsigned a = (av ^ ((i & 3u) << 6)) + 4096u * (i >> 2); return lds_tr2(a, a + 2048u); }

template <int T0, int T1> DI void qphase(Ctx& c, f32x16 (&acc)[4], unsigned so0, unsigned so1) {
  const unsigned ax = c.lbX + c.cs; c.cs = nxt(c.cs);
  const unsigned aw = c.lbW + c.cs; c.cs = nxt(c.cs);
  unsigned s0 = 0, s1 = 0;
  if (T0 >= 0) { s0 = c.is; c.is = nxt(c.is); }
  if (T1 >= 0) { s1 = c.is; c.is = nxt(c.is); }
  bf16x8 xb[4], af[16];
  xb[0] = lds_b128(ax);
#pragma unroll
  for (unsigned i = 0; i < 4; ++i) { const unsigned a = (aw ^ (i << 6)); af[i] = lds_tr2(a, a + 1024u); }
  xb[1] = lds_b128(ax ^ 32u);
  SB();
#pragma unroll
  for (unsigned i = 0; i < 16; i += 2) {
    if (i + 4 < 16) {
      const unsigned a = (aw ^ (((i + 4) & 3u) << 6)) + 4096u * ((i + 4) >> 2); af[i + 4] = lds_tr2(a, a + 1024u);
      const unsigned a2 = (aw ^ (((i + 5) & 3u) << 6)) + 4096u * ((i + 5) >> 2); af[i + 5] = lds_tr2(a2, a2 + 1024u);
    }
    if (i == 2) xb[2] = lds_b128(ax ^ 64u);
    if (i == 6) xb[3] = lds_b128(ax ^ 96u);
    SB();
    acc[i & 3] = MFMA(af[i], xb[i >> 2], acc[i & 3]);
    SB();
    if (i < 8) { if (T0 >= 0) piece_rt<(T0 >= 0 ? T0 : 0)>(c, i >> 1, s0, so0); }
    else { if (T1 >= 0) piece_rt<(T1 >= 0 ? T1 : 0)>(c, (i - 8) >> 1, s1, so1); }
    SB();
    acc[(i + 1) & 3] = MFMA(af[i + 1], xb[i >> 2], acc[(i + 1) & 3]);
    SB();
  }
}

template <bool FIN, bool KPRE, int T0, int T1>
DI void aphase(Ctx& c, const bf16x8 (&qf)[8], f32x16& n0, f32x16& n1, f32x16& p0, f32x16& p1, u32x4 (&pw)[4],
               bf16x8 (&pre)[4], float mc, float& l, unsigned so0, unsigned so1) {
  const unsigned ak = c.lbK + c.cs; c.cs = nxt(c.cs);
  const unsigned av = c.lbV + c.cs;
  unsigned s0 = 0, s1 = 0;
  if (T0 >= 0) { s0 = c.is; c.is = nxt(c.is); }
  if (T1 >= 0) { s1 = c.is; c.is = nxt(c.is); }
  bf16x8 kf[16];
  if (KPRE) { kf[0] = pre[0]; kf[1] = pre[1]; kf[2] = pre[2]; kf[3] = pre[3]; }
  else { kf[0] = KFRAG(ak, 0); kf[1] = KFRAG(ak, 1); kf[2] = KFRAG(ak, 2); kf[3] = KFRAG(ak, 3); }
  float ps0 = 0.f, ps1 = 0.f;
  SB();
#pragma unroll
  for (unsigned i = 0; i < 16; i += 2) {
    if (i + 4 < 16) { kf[i + 4] = KFRAG(ak, i + 4); kf[i + 5] = KFRAG(ak, i + 5); }
    else if (FIN) { pre[i - 12] = vfrag(av, i - 12); pre[i - 11] = vfrag(av, i - 11); }
    SB();
    if (i == 0) { f32x16 z = {}; n0 = MFMA(kf[i], qf[i >> 1], z); } else n0 = MFMA(kf[i], qf[i >> 1], n0);
    SB();
    if (FIN) {
      EXP2(p1, i); EXP2(p1, i + 1);
      pw[i >> 3][(i >> 1) & 3] = cvtpk(p0[i], p0[i + 1]);
    }
    if (i < 8) { if (T0 >= 0) piece_rt<(T0 >= 0 ? T0 : 0)>(c, i >> 1, s0, so0); }
    else { if (T1 >= 0) piece_rt<(T1 >= 0 ? T1 : 0)>(c, (i - 8) >> 1, s1, so1); }
    SB();
    if (i == 0) { f32x16 z = {}; n1 = MFMA(kf[i + 1], qf[i >> 1], z); } else n1 = MFMA(kf[i + 1], qf[i >> 1], n1);
    SB();
    if (FIN) {
      pw[2 + (i >> 3)][(i >> 1) & 3] = cvtpk(p1[i], p1[i + 1]);
      ps0 += p1[i]; ps1 += p1[i + 1];
    }
    SB();
  }
  if (FIN) l += ps0 + ps1;
}

template <bool FIRST> DI void sm_max(f32x16& s0, f32x16& s1, float& m, float& l, float& mc, float& alpha, bool& resc) {
  if (FIRST) asm volatile("s_nop 7\n\ts_nop 7" : "+v"(s0), "+v"(s1));
  float tm0 = max3f(s0[0], s0[1], s0[2]), tm1 = max3f(s1[0], s1[1], s1[2]);
#pragma unroll
  for (int r = 3; r < 15; r += 2) { tm0 = max3f(tm0, s0[r], s0[r + 1]); tm1 = max3f(tm1, s1[r], s1[r + 1]); }
  float tmax = max3f(tm0, tm1, s0[15]);
  tmax = max3f(tmax, s1[15], s1[15]);
  auto r = __builtin_amdgcn_permlane32_swap(__float_as_uint(tmax), __float_as_uint(tmax), false, false);
  tmax = max3f(__uint_as_float(r[0]), __uint_as_float(r[1]), __uint_as_float(r[1]));
  resc = false;
  if (FIRST) {
    m = tmax; mc = -m * SMC;
  } else if (__any(tmax - m > THRS)) {
    const float mn = fmaxf(m, tmax);
    alpha = __builtin_amdgcn_exp2f((m - mn) * SMC);
    m = mn; l *= alpha; mc = -m * SMC; resc = true;
  }
}

template <bool LAST, bool KNEXT, bool VNEXT, int T0>
DI void bphase(Ctx& c, f32x16 (&o)[4], f32x16& s0, f32x16& s1, u32x4 (&pw)[4], bf16x8 (&pre)[4], float& m, float& l, float& mc,
               unsigned so0) {
  const unsigned av = c.lbV + c.cs; c.cs = nxt(c.cs);
  const unsigned an = (KNEXT ? c.lbK : c.lbV) + c.cs;
  unsigned sl0 = 0;
  if (T0 >= 0) { sl0 = c.is; c.is = nxt(c.is); }
  bf16x8 vf[16];
  vf[0] = pre[0]; vf[1] = pre[1]; vf[2] = pre[2]; vf[3] = pre[3];
  float ps0 = 0.f, ps1 = 0.f, alpha = 1.f;
  bool resc = false;
  if (LAST) {
#pragma unroll
    for (int e = 0; e < 8; ++e) pw[e >> 2][e & 3] = cvtpk(s0[2 * e], s0[2 * e + 1]);
  }
  SB();
#pragma unroll
  for (unsigned i = 0; i < 16; i += 2) {
    const unsigned ks = i >> 2, db = i & 3u;
    if (i + 4 < 16) { vf[i + 4] = vfrag(av, i + 4); vf[i + 5] = vfrag(av, i + 5); }
    else if (KNEXT) { pre[i - 12] = KFRAG(an, i - 12); pre[i - 11] = KFRAG(an, i - 11); }
    else if (VNEXT) { pre[i - 12] = vfrag(an, i - 12); pre[i - 11] = vfrag(an, i - 11); }
    SB();
    o[db] = MFMA(vf[i], __builtin_bit_cast(bf16x8, pw[ks]), o[db]);
    SB();
    if (i < 8 && T0 >= 0) piece_rt<(T0 >= 0 ? T0 : 0)>(c, i >> 1, sl0, so0);
    if (LAST) {
      if (i < 8) {
        EXP2(s1, 2 * i); EXP2(s1, 2 * i + 1);
        pw[2 + (i >> 2)][i & 3] = cvtpk(s1[2 * i], s1[2 * i + 1]);
        ps0 += s1[2 * i]; ps1 += s1[2 * i + 1];
      }
    } else {
      if (i == 2) sm_max<false>(s0, s1, m, l, mc, alpha, resc);
      if (i >= 4) { const int r = (i - 4) * 4 / 3; (void)r; }
      if (i >= 6) { EXP2(s0, (i - 6) / 2 * 3); EXP2(s0, (i - 6) / 2 * 3 + 1); ps0 += s0[(i - 6) / 2 * 3]; ps1 += s0[(i - 6) / 2 * 3 + 1]; }
    }
    SB();
    o[db + 1] = MFMA(vf[i + 1], __builtin_bit_cast(bf16x8, pw[ks]), o[db + 1]);
    SB();
    if (LAST) {
      if (i < 8) {
        EXP2(s1, 2 * i + 2); EXP2(s1, 2 * i + 3);
        pw[2 + (i >> 2)][(i & 3) + 1] = cvtpk(s1[2 * i + 2], s1[2 * i + 3]);
        ps0 += s1[2 * i + 2]; ps1 += s1[2 * i + 3];
      }
    } else {
      if (i >= 6) { EXP2(s0, (i - 6) / 2 * 3 + 2); ps0 += s0[(i - 6) / 2 * 3 + 2]; }
      if (i == 4) { EXP2(s0, 15); ps1 += s0[15]; }
    }
    SB();
  }
  l += ps0 + ps1;
  if (!LAST) {
    if (resc) {
#pragma unroll
      for (int db = 0; db < 4; ++db)
#pragma unroll
        for (int r = 0; r < 16; ++r) o[db][r] *= alpha;
    }
  }
}

__global__ __launch_bounds__(256, 2) void attn_kernel(
    const __bf16* __restrict__ xA, const __bf16* __restrict__ kA, const __bf16* __restrict__ vA, const __bf16* __restrict__ wqA,
    const __bf16* __restrict__ xB, const __bf16* __restrict__ kB, const __bf16* __restrict__ vB, const __bf16* __restrict__ wqB,
    __bf16* __restrict__ ows) {
  extern __shared__ __attribute__((aligned(16))) char smem[];
  const unsigned ldsb = (unsigned)(size_t)(LAS char*)smem;
  const unsigned tid = threadIdx.x, lane = tid & 63u;
  const unsigned w = __builtin_amdgcn_readfirstlane(tid >> 6);
  const unsigned bid = blockIdx.x;
  const unsigned typeB = (__builtin_amdgcn_s_getreg(6 | (11 << 11)) != 0) ? 1u : 0u;
  const unsigned xcd = bid & 7u, idx = bid >> 3;
  const unsigned br = xcd >> 2, h = (xcd & 3u) * 8u + (idx & 7u), b0 = idx >> 3;
  Ctx c;
  c.rx = __builtin_amdgcn_make_buffer_rsrc((void*)(br ? xB : xA), 0, NB * SQ * DM * 2, 0x00020000);
  c.rw = __builtin_amdgcn_make_buffer_rsrc((void*)((br ? wqB : wqA) + (size_t)h * DH), 0, ((DM - 1) * NH * DH + DH) * 2, 0x00020000);
  c.rk = __builtin_amdgcn_make_buffer_rsrc((void*)(br ? kB : kA), 0, NB * NH * SK * DH * 2, 0x00020000);
  c.rv = __builtin_amdgcn_make_buffer_rsrc((void*)(br ? vB : vA), 0, NB * NH * SK * DH * 2, 0x00020000);
  const unsigned l4 = lane >> 4, l15 = lane & 15u;
  c.voffK = (16u * w + l4) * 256u + ((l15 ^ l4) << 4);
  c.voffV = (16u * w + l4) * 256u + ((l15 ^ (l4 << 2)) << 4);
  c.voffW = (16u * w + l4) * 8192u + ((l15 ^ (l4 << 2)) << 4);
  c.voffX = (32u * w + (lane >> 3)) * 1024u + (((lane & 7u) ^ l4) << 4);
  const unsigned r32 = lane & 31u, hh = lane >> 5;
  const unsigned q4 = (lane >> 2) & 3u, p4 = lane & 3u, dhalf = (lane >> 4) & 1u;
  c.lbK = ldsb + r32 * 256u + ((hh ^ (r32 & 15u)) << 4);
  c.lbV = ldsb + (4u * hh + q4) * 256u + (q4 << 6) + 32u * dhalf + 8u * p4;
  const unsigned psw = ((p4 & 1u) << 1) | (p4 >> 1);
  c.lbW = ldsb + (8u * hh + q4) * 256u + (q4 << 6) + 32u * dhalf + 8u * psw;
  c.lbX = ldsb + (32u * w + r32) * 128u + ((hh ^ ((r32 >> 1) & 7u)) << 4);
  c.m0w = ldsb + w * 4096u;
  c.is = 0; c.cs = 0;
  constexpr unsigned WSTEP = 64u * 8192u, OOB = 0x7f000000u;

  tile_all<TX>(c, b0 * (SQ * DM * 2)); tile_all<TW>(c, 0); tile_all<TX>(c, b0 * (SQ * DM * 2) + 128u); tile_all<TW>(c, WSTEP);
  if (!typeB) __builtin_amdgcn_s_setprio(2);

  for (unsigned unit = 0; unit < 2; ++unit) {
    const unsigned b = b0 + 8u * unit;
    const unsigned xso = b * (SQ * DM * 2);
    const unsigned kv = (b * NH + h) * (SK * DH * 2);
    const unsigned xnx = unit == 0 ? xso + 8u * (SQ * DM * 2) : OOB;
    const unsigned wnx = unit == 0 ? 0u : OOB;
    bf16x8 qf[8];
    {
      f32x16 acc[4] = {};
      if (unit == 0) WAITV(8); else WAITV(16);
      BARRIER();
      qphase<-1, TX>(c, acc, 0, xso + 256u);
      if (unit == 0) WAITV(4); else WAITV(12);
      BARRIER();
      qphase<TW, TX>(c, acc, 2u * WSTEP, xso + 384u);
      for (unsigned j = 2; j < 6; ++j) {
        WAITV(4); BARRIER();
        qphase<TW, TX>(c, acc, (j + 1u) * WSTEP, xso + (j + 2u) * 128u);
      }
      WAITV(4); BARRIER();
      qphase<TW, TK>(c, acc, 7u * WSTEP, kv);
      WAITV(4); BARRIER();
      qphase<TK, TV>(c, acc, kv + 16384u, kv);
#pragma unroll
      for (int db = 0; db < 4; ++db) { qf[2 * db] = pack8n(acc[db], 0); qf[2 * db + 1] = pack8n(acc[db], 8); }
    }
    __builtin_amdgcn_s_setprio(0);
    f32x16 o[4] = {};
    f32x16 sA0, sA1, sB0, sB1;
    u32x4 pw[4];
    bf16x8 pre[4];
    float m, l = 0.f, mc;
    WAITV(8); BARRIER();
    aphase<false, false, TK, TV>(c, qf, sA0, sA1, sB0, sB1, pw, pre, 0.f, l, kv + 2u * 16384u, kv + 16384u);
    {
      float alpha; bool resc;
      sm_max<true>(sA0, sA1, m, l, mc, alpha, resc);
      float ps0 = 0.f, ps1 = 0.f;
#pragma unroll
      for (int r = 0; r < 16; r += 2) { EXP2(sA0, r); EXP2(sA0, r + 1); ps0 += sA0[r]; ps1 += sA0[r + 1]; }
      l += ps0 + ps1;
    }
    WAITV(8); BARRIER();
    aphase<true, false, TK, -1>(c, qf, sB0, sB1, sA0, sA1, pw, pre, mc, l, kv + 3u * 16384u, 0);
    WAITV(8); BARRIER();
    bphase<false, true, false, TV>(c, o, sB0, sB1, pw, pre, m, l, mc, kv + 2u * 16384u);
    for (unsigned t = 2; t < 6; t += 2) {
      WAITV(8); BARRIER();
      aphase<true, true, TK, -1>(c, qf, sA0, sA1, sB0, sB1, pw, pre, mc, l, kv + (t + 2u) * 16384u, 0);
      WAITV(8); BARRIER();
      bphase<false, true, false, TV>(c, o, sA0, sA1, pw, pre, m, l, mc, kv + (t + 1u) * 16384u);
      WAITV(8); BARRIER();
      aphase<true, true, TK, -1>(c, qf, sB0, sB1, sA0, sA1, pw, pre, mc, l, kv + (t + 3u) * 16384u, 0);
      WAITV(8); BARRIER();
      bphase<false, true, false, TV>(c, o, sB0, sB1, pw, pre, m, l, mc, kv + (t + 2u) * 16384u);
    }
    WAITV(8); BARRIER();
    aphase<true, true, TV, -1>(c, qf, sA0, sA1, sB0, sB1, pw, pre, mc, l, kv + 7u * 16384u, 0);
    WAITV(8); BARRIER();
    bphase<false, true, false, TX>(c, o, sA0, sA1, pw, pre, m, l, mc, xnx);
    WAITV(8); BARRIER();
    aphase<true, true, TW, -1>(c, qf, sB0, sB1, sA0, sA1, pw, pre, mc, l, wnx, 0);
    WAITV(8); BARRIER();
    bphase<false, false, true, TX>(c, o, sB0, sB1, pw, pre, m, l, mc, xnx + 128u);
    WAITV(12); BARRIER();
    bphase<true, false, false, TW>(c, o, sB0, sB1, pw, pre, m, l, mc, wnx + WSTEP);
    const float inv = 1.0f / xhalf_sum(l);
    const unsigned u = (br * NB + b) * NH + h;
    bf16x8* op = (bf16x8*)ows + ((size_t)(u * 4u + w) * 8u) * 64u + lane;
#pragma unroll
    for (int db = 0; db < 4; ++db) {
      op[(2 * db) * 64] = pack8(o[db], 0, inv);
      op[(2 * db + 1) * 64] = pack8(o[db], 8, inv);
    }
    asm volatile("" ::: "memory");
  }
}

template <int IMM> DI void ldo(bf16x8& d, unsigned voff, const void* base) {
  asm volatile("global_load_dwordx4 %0, %1, %2 offset:%3" : "=v"(d) : "v"(voff), "s"(base), "i"(IMM) : "memory");
}
DI void ldo_rt(bf16x8 (&S)[8], int i, unsigned vo, unsigned vo2, const void* base) {
  if (i == 0) ldo<0>(S[0], vo, base);
  if (i == 1) ldo<1024>(S[1], vo, base);
  if (i == 2) ldo<2048>(S[2], vo, base);
  if (i == 3) ldo<3072>(S[3], vo, base);
  if (i == 4) ldo<0>(S[4], vo2, base);
  if (i == 5) ldo<1024>(S[5], vo2, base);
  if (i == 6) ldo<2048>(S[6], vo2, base);
  if (i == 7) ldo<3072>(S[7], vo2, base);
}
#define WAITO(n, S)                                                                       \
  asm volatile("s_waitcnt vmcnt(" #n ") lgkmcnt(0)\n\ts_barrier"                          \
               : "+v"(S[0]), "+v"(S[1]), "+v"(S[2]), "+v"(S[3]), "+v"(S[4]), "+v"(S[5]),  \
                 "+v"(S[6]), "+v"(S[7])::"memory")
DI void wo_piece(int i, unsigned m0v, unsigned v, rsrc_t r, unsigned so) {
  if (i == 0) dma1<0, 0>(m0v, v, r, so);
  if (i == 1) dma1<1, 0>(m0v, v + 7168u, r, so);
  if (i == 2) dma1<2, 0>(m0v, v + 14336u, r, so);
  if (i == 3) dma1<3, 0>(m0v, v + 21504u, r, so);
}
template <bool ISSUE>
DI void ostep(f32x16& acc0, f32x16& acc1, const bf16x8 (&SC)[8], bf16x8 (&SN)[8], unsigned a0, unsigned m0v, unsigned voffWo,
              rsrc_t rwo, unsigned so, unsigned vo, unsigned vo2, const char* onext) {
  bf16x8 f[16];
  const unsigned a1 = a0 ^ 64u;
#define OFRAG(i) lds_tr2((((i) & 1) ? a1 : a0) + 2048u * ((i) >> 1), (((i) & 1) ? a1 : a0) + 2048u * ((i) >> 1) + 1024u)
  f[0] = OFRAG(0); f[1] = OFRAG(1); f[2] = OFRAG(2); f[3] = OFRAG(3);
  SB();
#pragma unroll
  for (unsigned i = 0; i < 16; i += 2) {
    if (i + 4 < 16) { f[i + 4] = OFRAG(i + 4); f[i + 5] = OFRAG(i + 5); }
    SB();
    acc0 = MFMA(f[i], SC[i >> 1], acc0);
    SB();
    if (ISSUE) { if (i < 8) wo_piece(i >> 1, m0v, voffWo, rwo, so); }
    SB();
    acc1 = MFMA(f[i + 1], SC[i >> 1], acc1);
    SB();
    if (ISSUE) ldo_rt(SN, i >> 1, vo, vo2, onext);
    SB();
  }
#undef OFRAG
}

__global__ __launch_bounds__(512, 2) void oproj_kernel(const __bf16* __restrict__ ows, const __bf16* __restrict__ woA,
                                                       const __bf16* __restrict__ woB, __bf16* __restrict__ out) {
  extern __shared__ __attribute__((aligned(16))) char smem[];
  const unsigned ldsb = (unsigned)(size_t)(LAS char*)smem;
  const unsigned tid = threadIdx.x, lane = tid & 63u;
  const unsigned w8 = __builtin_amdgcn_readfirstlane(tid >> 6);
  const unsigned w = w8 & 3u, g = w8 >> 2;
  const unsigned bid = blockIdx.x;
  const unsigned xcd = bid & 7u, idx = bid >> 3;
  const unsigned pair = xcd * 4u + (idx >> 3), nt = idx & 7u;
  const unsigned br = pair >> 4, b = pair & 15u;
  const __bf16* wo = (br ? woB : woA) + nt * 64u;
  const rsrc_t rwo = __builtin_amdgcn_make_buffer_rsrc((void*)wo, 0, ((NH * DH - 1) * DM + 64) * 2, 0x00020000);
  const unsigned voffWo = (32u * w + (lane >> 3)) * 1024u + (((lane & 7u) ^ (((lane >> 4) & 1u) << 2)) << 4);
  const unsigned hh = lane >> 5, q4 = (lane >> 2) & 3u, p4 = lane & 3u, dhalf = (lane >> 4) & 1u;
  const unsigned coloff = 16u * (p4 & 1u) + 8u * dhalf + 4u * (p4 >> 1);
  const unsigned ring = ldsb + g * (4u * SLOT);
  const unsigned lbA = ring + (4u * hh + q4) * 128u + (((q4 >> 1) & 1u) << 6) + 2u * coloff;
  const unsigned m0w = ring + w * 4096u;
  const char* obase = (const char*)ows + ((size_t)(pair * NH + 16u * g) * 4u + w) * 8192u;
  const unsigned wso = 16u * g * 131072u;
  const unsigned vo = lane * 16u, vo2 = lane * 16u + 4096u;

  bf16x8 B0[8], B1[8], B2[8], B3[8];
  f32x16 acc0 = {}, acc1 = {};
#pragma unroll
  for (int i = 0; i < 4; ++i) wo_piece(i, m0w, voffWo, rwo, wso);
#pragma unroll
  for (int i = 0; i < 8; ++i) ldo_rt(B0, i, vo, vo2, obase);
#pragma unroll
  for (int i = 0; i < 4; ++i) wo_piece(i, m0w + SLOT, voffWo, rwo, wso + 131072u);
#pragma unroll
  for (int i = 0; i < 8; ++i) ldo_rt(B1, i, vo, vo2, obase + 32768);
#pragma unroll
  for (int i = 0; i < 4; ++i) wo_piece(i, m0w + 2 * SLOT, voffWo, rwo, wso + 2u * 131072u);
#pragma unroll
  for (int i = 0; i < 8; ++i) ldo_rt(B2, i, vo, vo2, obase + 2 * 32768);

#define STEP(ISS, hd, SC, SN, WN)                                                          \
  do {                                                                                     \
    WAITO(WN, SC);                                                                         \
    ostep<ISS>(acc0, acc1, SC, SN, lbA + ((hd) & 3u) * SLOT, m0w + (((hd) + 3u) & 3u) * SLOT, voffWo, rwo, \
               wso + ((hd) + 3u) * 131072u, vo, vo2, obase + (size_t)((hd) + 3u) * 32768u); \
  } while (0)
  for (unsigned hd = 0; hd < 12; hd += 4) {
    STEP(true, hd, B0, B3, 24);
    STEP(true, hd + 1, B1, B0, 24);
    STEP(true, hd + 2, B2, B1, 24);
    STEP(true, hd + 3, B3, B2, 24);
  }
  STEP(true, 12u, B0, B3, 24);
  STEP(false, 13u, B1, B0, 24);
  STEP(false, 14u, B2, B1, 12);
  STEP(false, 15u, B3, B2, 0);
#undef STEP
  BARRIER();
  typedef __attribute__((ext_vector_type(4))) float f32x4;
  LAS f32x4* red = (LAS f32x4*)(ldsb + w * 8192u) + lane;
  if (g == 1) {
#pragma unroll
    for (int i = 0; i < 4; ++i) {
      red[i * 64] = f32x4{acc0[4 * i], acc0[4 * i + 1], acc0[4 * i + 2], acc0[4 * i + 3]};
      red[(4 + i) * 64] = f32x4{acc1[4 * i], acc1[4 * i + 1], acc1[4 * i + 2], acc1[4 * i + 3]};
    }
  }
  BARRIER();
  if (g == 0) {
#pragma unroll
    for (int i = 0; i < 4; ++i) {
      f32x4 a = red[i * 64], c2 = red[(4 + i) * 64];
#pragma unroll
      for (int e = 0; e < 4; ++e) { acc0[4 * i + e] += a[e]; acc1[4 * i + e] += c2[e]; }
    }
    const unsigned r32 = lane & 31u;
    __bf16* orow = out + ((size_t)b * SQ + 32u * w + r32) * 1024u + br * 512u + nt * 64u + 16u * hh;
    *(bf16x8*)(orow) = pack8n(acc0, 0);
    *(bf16x8*)(orow + 8) = pack8n(acc0, 8);
    *(bf16x8*)(orow + 32) = pack8n(acc1, 0);
    *(bf16x8*)(orow + 40) = pack8n(acc1, 8);
  }
}

extern "C" void kernel_launch(void* const* d_in, const int* in_sizes, int n_in, void* d_out, int out_size, void* d_ws,
                              size_t ws_size, hipStream_t stream) {
  const __bf16* xA = (const __bf16*)d_in[0];
  const __bf16* kA = (const __bf16*)d_in[1];
  const __bf16* vA = (const __bf16*)d_in[2];
  const __bf16* wqA = (const __bf16*)d_in[3];
  const __bf16* woA = (const __bf16*)d_in[4];
  const __bf16* xB = (const __bf16*)d_in[5];
  const __bf16* kB = (const __bf16*)d_in[6];
  const __bf16* vB = (const __bf16*)d_in[7];
  const __bf16* wqB = (const __bf16*)d_in[8];
  const __bf16* woB = (const __bf16*)d_in[9];
  __bf16* ows = (__bf16*)d_ws;
  hipFuncSetAttribute((const void*)attn_kernel, hipFuncAttributeMaxDynamicSharedMemorySize, NSLOT * SLOT);
  hipFuncSetAttribute((const void*)oproj_kernel, hipFuncAttributeMaxDynamicSharedMemorySize, 8 * SLOT);
  hipLaunchKernelGGL(attn_kernel, dim3(512), dim3(256), NSLOT * SLOT, stream, xA, kA, vA, wqA, xB, kB, vB, wqB, ows);
  hipLaunchKernelGGL(oproj_kernel, dim3(256), dim3(512), 8 * SLOT, stream, (const __bf16*)ows, woA, woB, (__bf16*)d_out);
}
```

```cpp
#include <hip/hip_runtime.h>
#include <hip/hip_bf16.h>
#include <stdint.h>

typedef __attribute__((ext_vector_type(8))) __bf16 bf16x8;
typedef __attribute__((ext_vector_type(4))) __bf16 bf16x4;
typedef __attribute__((ext_vector_type(2))) __bf16 bf16x2;
typedef __attribute__((ext_vector_type(16))) float f32x16;
typedef __attribute__((ext_vector_type(2))) float f32x2;
typedef __attribute__((ext_vector_type(4))) unsigned u32x4;
#define LAS __attribute__((address_space(3)))
#define DI __device__ __forceinline__
#define AI __attribute__((always_inline))
typedef __amdgpu_buffer_rsrc_t rsrc_t;

constexpr int NB = 16, SQ = 128, DM = 512, NH = 32, SK = 512, DH = 128;
constexpr unsigned SLOT = 16384, NSLOT = 5;

#define WAITV(n) asm volatile("s_waitcnt vmcnt(" #n ")" ::: "memory")
#define BARRIER() asm volatile("s_waitcnt lgkmcnt(0)\n\ts_barrier" ::: "memory")

DI void dma16(unsigned m0v, unsigned voff, rsrc_t r, unsigned soff) {
  asm volatile("s_mov_b32 m0, %0\n\ts_nop 0\n\tbuffer_load_dwordx4 %1, %2, %3 offen lds"
               :: "s"(m0v), "v"(voff), "s"(r), "s"(soff) : "memory");
}
DI void dma16nt(unsigned m0v, unsigned voff, rsrc_t r, unsigned soff) {
  asm volatile("s_mov_b32 m0, %0\n\ts_nop 0\n\tbuffer_load_dwordx4 %1, %2, %3 offen nt lds"
               :: "s"(m0v), "v"(voff), "s"(r), "s"(soff) : "memory");
}
DI unsigned cvtpk(float lo, float hi) {
  f32x2 v = {lo, hi};
  bf16x2 b = __builtin_convertvector(v, bf16x2);
  return __builtin_bit_cast(unsigned, b);
}
DI bf16x8 pack8(const f32x16& a, int base, float sc) {
  u32x4 w = {cvtpk(a[base + 0] * sc, a[base + 1] * sc), cvtpk(a[base + 2] * sc, a[base + 3] * sc),
             cvtpk(a[base + 4] * sc, a[base + 5] * sc), cvtpk(a[base + 6] * sc, a[base + 7] * sc)};
  return __builtin_bit_cast(bf16x8, w);
}
DI bf16x8 pack8n(const f32x16& a, int base) {
  u32x4 w = {cvtpk(a[base + 0], a[base + 1]), cvtpk(a[base + 2], a[base + 3]),
             cvtpk(a[base + 4], a[base + 5]), cvtpk(a[base + 6], a[base + 7])};
  return __builtin_bit_cast(bf16x8, w);
}
DI bf16x8 lds_b128(unsigned addr) { return *(LAS const bf16x8*)(addr); }
DI bf16x8 lds_tr2(unsigned addr0, unsigned addr1) {
  bf16x4 lo = __builtin_amdgcn_ds_read_tr16_b64_v4bf16((LAS bf16x4*)(addr0));
  bf16x4 hi = __builtin_amdgcn_ds_read_tr16_b64_v4bf16((LAS bf16x4*)(addr1));
  return __builtin_shufflevector(lo, hi, 0, 1, 2, 3, 4, 5, 6, 7);
}
DI float xhalf_max(float x) {
  auto r = __builtin_amdgcn_permlane32_swap(__float_as_uint(x), __float_as_uint(x), false, false);
  return fmaxf(__uint_as_float(r[0]), __uint_as_float(r[1]));
}
DI float xhalf_sum(float x) {
  auto r = __builtin_amdgcn_permlane32_swap(__float_as_uint(x), __float_as_uint(x), false, false);
  return __uint_as_float(r[0]) + __uint_as_float(r[1]);
}
#define MFMA(a, b, c) __builtin_amdgcn_mfma_f32_32x32x16_bf16((a), (b), (c), 0, 0, 0)

#define SB() __builtin_amdgcn_sched_barrier(0)
DI float max3f(float a, float b, float c) { float r; asm("v_max3_f32 %0, %1, %2, %3" : "=v"(r) : "v"(a), "v"(b), "v"(c)); return r; }
template <int I, int NT> DI void dma1(unsigned m0v, unsigned v, rsrc_t r, unsigned soff) {
  if (NT) asm volatile("s_mov_b32 m0, %0\n\ts_nop 0\n\tbuffer_load_dwordx4 %1, %2, %3 offen offset:%4 nt lds"
                       :: "s"(m0v), "v"(v), "s"(r), "s"(soff), "i"(I * 1024) : "memory");
  else asm volatile("s_mov_b32 m0, %0\n\ts_nop 0\n\tbuffer_load_dwordx4 %1, %2, %3 offen offset:%4 lds"
                    :: "s"(m0v), "v"(v), "s"(r), "s"(soff), "i"(I * 1024) : "memory");
}
template <int I> DI void xld(bf16x8& d, unsigned v, rsrc_t r, unsigned soff) {
  asm volatile("buffer_load_dwordx4 %0, %1, %2, %3 offen offset:%4" : "=v"(d) : "v"(v), "s"(r), "s"(soff), "i"(I * 32) : "memory");
}
enum { TW = 1, TK = 2, TV = 3 };

struct Ctx {
  rsrc_t rx, rw, rk, rv;
  unsigned voffK, voffV, voffW, voffX, lbK, lbV, lbW, m0w;
  unsigned is, cs;
};
DI unsigned nxt(unsigned s) { return s == (NSLOT - 1) * SLOT ? 0u : s + SLOT; }
template <int T, int I> DI void piece(const Ctx& c, unsigned slot, unsigned so) {
  if (T == TW) dma1<I, 0>(c.m0w + slot, c.voffW + I * 31744u, c.rw, so);
  if (T == TK) dma1<I, 1>(c.m0w + slot, c.voffK ^ (I << 6), c.rk, so);
  if (T == TV) dma1<I, 1>(c.m0w + slot, c.voffV, c.rv, so);
}
template <int T> DI void piece_rt(const Ctx& c, int i, unsigned slot, unsigned so) {
  if (i == 0) piece<T, 0>(c, slot, so);
  if (i == 1) piece<T, 1>(c, slot, so);
  if (i == 2) piece<T, 2>(c, slot, so);
  if (i == 3) piece<T, 3>(c, slot, so);
}
DI void xld_rt(const Ctx& c, bf16x8 (&x)[4], int i, unsigned so) {
  if (i == 0) xld<0>(x[0], c.voffX, c.rx, so);
  if (i == 1) xld<1>(x[1], c.voffX, c.rx, so);
  if (i == 2) xld<2>(x[2], c.voffX, c.rx, so);
  if (i == 3) xld<3>(x[3], c.voffX, c.rx, so);
}
template <int T> DI void tile_all(Ctx& c, unsigned so) {
  piece<T, 0>(c, c.is, so); piece<T, 1>(c, c.is, so); piece<T, 2>(c, c.is, so); piece<T, 3>(c, c.is, so);
  c.is = nxt(c.is);
}
DI void xld_all(const Ctx& c, bf16x8 (&x)[4], unsigned so) {
  xld<0>(x[0], c.voffX, c.rx, so); xld<1>(x[1], c.voffX, c.rx, so); xld<2>(x[2], c.voffX, c.rx, so); xld<3>(x[3], c.voffX, c.rx, so);
}
#define SYNCX(n, X) asm volatile("s_waitcnt vmcnt(" #n ") lgkmcnt(0)\n\ts_barrier" : "+v"(X[0]), "+v"(X[1]), "+v"(X[2]), "+v"(X[3]) :: "memory")
#define SYNC(n) asm volatile("s_waitcnt vmcnt(" #n ") lgkmcnt(0)\n\ts_barrier" ::: "memory")

constexpr float SMC = 0.08838834764831845f * 1.4426950408889634f;
constexpr float THRS = 8.0f / SMC;
#define EXP2(S, R) S[R] = __builtin_amdgcn_exp2f(fmaf(S[R], SMC, mc))
DI bf16x8 kfrag(unsigned a, unsigned i) { return lds_b128((a ^ ((i >> 1) << 5)) + 8192u * (i & 1u)); }
DI bf16x8 vfrag(unsigned av, unsigned i) { const unsigned a = (av ^ ((i & 3u) << 6)) + 4096u * (i >> 2); return lds_tr2(a, a + 2048u); }
DI bf16x8 wfrag(unsigned aw, unsigned i) { const unsigned a = (aw ^ ((i & 3u) << 6)) + 4096u * (i >> 2); return lds_tr2(a, a + 1024u); }
template <int T> DI bf16x8 nfrag(const Ctx& c, unsigned slot, unsigned i) {
  if (T == TW) return wfrag(c.lbW + slot, i);
  if (T == TK) return kfrag(c.lbK + slot, i);
  return vfrag(c.lbV + slot, i);
}

template <int T0, bool XL, int TN>
DI void qphase(Ctx& c, f32x16 (&acc)[4], const bf16x8 (&xb)[4], bf16x8 (&xn)[4], bf16x8 (&pre)[4], unsigned so0, unsigned xso) {
  const unsigned aw = c.lbW + c.cs; c.cs = nxt(c.cs);
  const unsigned s0 = c.is; c.is = nxt(c.is);
  bf16x8 af[16];
  af[0] = pre[0]; af[1] = pre[1]; af[2] = pre[2]; af[3] = pre[3];
  SB();
#pragma unroll
  for (unsigned i = 0; i < 16; i += 2) {
    if (i + 4 < 16) { af[i + 4] = wfrag(aw, i + 4); af[i + 5] = wfrag(aw, i + 5); }
    else { pre[i - 12] = nfrag<TN>(c, c.cs, i - 12); pre[i - 11] = nfrag<TN>(c, c.cs, i - 11); }
    SB();
    acc[i & 3] = MFMA(af[i], xb[i >> 2], acc[i & 3]);
    SB();
    if (i < 8) piece_rt<T0>(c, i >> 1, s0, so0);
    else if (XL) xld_rt(c, xn, (i - 8) >> 1, xso);
    SB();
    acc[(i + 1) & 3] = MFMA(af[i + 1], xb[i >> 2], acc[(i + 1) & 3]);
    SB();
  }
}

template <bool FIN, int T0, bool XL>
DI void aphase(Ctx& c, const bf16x8 (&qf)[8], f32x16& n0, f32x16& n1, f32x16& p0, f32x16& p1, u32x4 (&pw)[4],
               bf16x8 (&pre)[4], float mc, float& l, unsigned so0, bf16x8 (&xn)[4], unsigned xso) {
  const unsigned ak = c.lbK + c.cs; c.cs = nxt(c.cs);
  const unsigned s0 = c.is; c.is = nxt(c.is);
  bf16x8 kf[16];
  kf[0] = pre[0]; kf[1] = pre[1]; kf[2] = pre[2]; kf[3] = pre[3];
  float ps0 = 0.f, ps1 = 0.f;
  SB();
#pragma unroll
  for (unsigned i = 0; i < 16; i += 2) {
    if (i + 4 < 16) { kf[i + 4] = kfrag(ak, i + 4); kf[i + 5] = kfrag(ak, i + 5); }
    else { pre[i - 12] = nfrag<(FIN ? TV : TK)>(c, c.cs, i - 12); pre[i - 11] = nfrag<(FIN ? TV : TK)>(c, c.cs, i - 11); }
    SB();
    if (i == 0) { f32x16 z = {}; n0 = MFMA(kf[i], qf[i >> 1], z); } else n0 = MFMA(kf[i], qf[i >> 1], n0);
    SB();
    if (FIN) {
      EXP2(p1, i); EXP2(p1, i + 1);
      pw[i >> 3][(i >> 1) & 3] = cvtpk(p0[i], p0[i + 1]);
    }
    if (i < 8) piece_rt<T0>(c, i >> 1, s0, so0);
    else if (XL) xld_rt(c, xn, (i - 8) >> 1, xso);
    SB();
    if (i == 0) { f32x16 z = {}; n1 = MFMA(kf[i + 1], qf[i >> 1], z); } else n1 = MFMA(kf[i + 1], qf[i >> 1], n1);
    SB();
    if (FIN) {
      pw[2 + (i >> 3)][(i >> 1) & 3] = cvtpk(p1[i], p1[i + 1]);
      ps0 += p1[i]; ps1 += p1[i + 1];
    }
    SB();
  }
  if (FIN) l += ps0 + ps1;
}

template <bool FIRST> DI void sm_max(f32x16& s0, f32x16& s1, float& m, float& l, float& mc, float& alpha, bool& resc) {
  if (FIRST) asm volatile("s_nop 7\n\ts_nop 7" : "+v"(s0), "+v"(s1));
  float tm0 = max3f(s0[0], s0[1], s0[2]), tm1 = max3f(s1[0], s1[1], s1[2]);
#pragma unroll
  for (int r = 3; r < 15; r += 2) { tm0 = max3f(tm0, s0[r], s0[r + 1]); tm1 = max3f(tm1, s1[r], s1[r + 1]); }
  float tmax = max3f(tm0, tm1, s0[15]);
  tmax = max3f(tmax, s1[15], s1[15]);
  auto r = __builtin_amdgcn_permlane32_swap(__float_as_uint(tmax), __float_as_uint(tmax), false, false);
  tmax = max3f(__uint_as_float(r[0]), __uint_as_float(r[1]), __uint_as_float(r[1]));
  resc = false;
  if (FIRST) {
    m = tmax; mc = -m * SMC;
  } else if (__any(tmax - m > THRS)) {
    const float mn = fmaxf(m, tmax);
    alpha = __builtin_amdgcn_exp2f((m - mn) * SMC);
    m = mn; l *= alpha; mc = -m * SMC; resc = true;
  }
}

template <bool LAST, int TN, int T0, bool XL>
DI void bphase(Ctx& c, f32x16 (&o)[4], f32x16& s0, f32x16& s1, u32x4 (&pw)[4], bf16x8 (&pre)[4], float& m, float& l, float& mc,
               unsigned so0, bf16x8 (&xn)[4], unsigned xso) {
  const unsigned av = c.lbV + c.cs; c.cs = nxt(c.cs);
  const unsigned sl0 = c.is; c.is = nxt(c.is);
  bf16x8 vf[16];
  vf[0] = pre[0]; vf[1] = pre[1]; vf[2] = pre[2]; vf[3] = pre[3];
  float ps0 = 0.f, ps1 = 0.f, alpha = 1.f;
  bool resc = false;
  if (LAST) {
#pragma unroll
    for (int e = 0; e < 8; ++e) pw[e >> 2][e & 3] = cvtpk(s0[2 * e], s0[2 * e + 1]);
  }
  SB();
#pragma unroll
  for (unsigned i = 0; i < 16; i += 2) {
    const unsigned ks = i >> 2, db = i & 3u;
    if (i + 4 < 16) { vf[i + 4] = vfrag(av, i + 4); vf[i + 5] = vfrag(av, i + 5); }
    else { pre[i - 12] = nfrag<TN>(c, c.cs, i - 12); pre[i - 11] = nfrag<TN>(c, c.cs, i - 11); }
    SB();
    o[db] = MFMA(vf[i], __builtin_bit_cast(bf16x8, pw[ks]), o[db]);
    SB();
    if (i < 8) piece_rt<T0>(c, i >> 1, sl0, so0);
    else if (XL) xld_rt(c, xn, (i - 8) >> 1, xso);
    if (LAST) {
      if (i < 8) {
        EXP2(s1, 2 * i); EXP2(s1, 2 * i + 1);
        pw[2 + (i >> 2)][i & 3] = cvtpk(s1[2 * i], s1[2 * i + 1]);
        ps0 += s1[2 * i]; ps1 += s1[2 * i + 1];
      }
    } else {
      if (i == 2) sm_max<false>(s0, s1, m, l, mc, alpha, resc);
      if (i >= 6) { EXP2(s0, (i - 6) / 2 * 3); EXP2(s0, (i - 6) / 2 * 3 + 1); ps0 += s0[(i - 6) / 2 * 3]; ps1 += s0[(i - 6) / 2 * 3 + 1]; }
    }
    SB();
    o[db + 1] = MFMA(vf[i + 1], __builtin_bit_cast(bf16x8, pw[ks]), o[db + 1]);
    SB();
    if (LAST) {
      if (i < 8) {
        EXP2(s1, 2 * i + 2); EXP2(s1, 2 * i + 3);
        pw[2 + (i >> 2)][(i & 3) + 1] = cvtpk(s1[2 * i + 2], s1[2 * i + 3]);
        ps0 += s1[2 * i + 2]; ps1 += s1[2 * i + 3];
      }
    } else {
      if (i >= 6) { EXP2(s0, (i - 6) / 2 * 3 + 2); ps0 += s0[(i - 6) / 2 * 3 + 2]; }
      if (i == 4) { EXP2(s0, 15); ps1 += s0[15]; }
    }
    SB();
  }
  l += ps0 + ps1;
  if (!LAST) {
    if (resc) {
#pragma unroll
      for (int db = 0; db < 4; ++db)
#pragma unroll
        for (int r = 0; r < 16; ++r) o[db][r] *= alpha;
    }
  }
}

__global__ __launch_bounds__(256, 2) void attn_kernel(
    const __bf16* __restrict__ xA, const __bf16* __restrict__ kA, const __bf16* __restrict__ vA, const __bf16* __restrict__ wqA,
    const __bf16* __restrict__ woA,
    const __bf16* __restrict__ xB, const __bf16* __restrict__ kB, const __bf16* __restrict__ vB, const __bf16* __restrict__ wqB,
    const __bf16* __restrict__ woB, __bf16* __restrict__ ows) {
  extern __shared__ __attribute__((aligned(16))) char smem[];
  const unsigned ldsb = (unsigned)(size_t)(LAS char*)smem;
  const unsigned tid = threadIdx.x, lane = tid & 63u;
  const unsigned w = __builtin_amdgcn_readfirstlane(tid >> 6);
  const unsigned bid = blockIdx.x;
  const unsigned typeB = (__builtin_amdgcn_s_getreg(6 | (11 << 11)) != 0) ? 1u : 0u;
  const unsigned xcd = bid & 7u, idx = bid >> 3;
  const unsigned br = xcd >> 2, h = (xcd & 3u) * 8u + (idx & 7u), b0 = idx >> 3;
  Ctx c;
  c.rx = __builtin_amdgcn_make_buffer_rsrc((void*)(br ? xB : xA), 0, NB * SQ * DM * 2, 0x00020000);
  c.rw = __builtin_amdgcn_make_buffer_rsrc((void*)((br ? wqB : wqA) + (size_t)h * DH), 0, ((DM - 1) * NH * DH + DH) * 2, 0x00020000);
  c.rk = __builtin_amdgcn_make_buffer_rsrc((void*)(br ? kB : kA), 0, NB * NH * SK * DH * 2, 0x00020000);
  c.rv = __builtin_amdgcn_make_buffer_rsrc((void*)(br ? vB : vA), 0, NB * NH * SK * DH * 2, 0x00020000);
  const unsigned l4 = lane >> 4, l15 = lane & 15u;
  c.voffK = (16u * w + l4) * 256u + ((l15 ^ l4) << 4);
  c.voffV = (16u * w + l4) * 256u + ((l15 ^ (l4 << 2)) << 4);
  c.voffW = (16u * w + l4) * 8192u + ((l15 ^ (l4 << 2)) << 4);
  const unsigned r32 = lane & 31u, hh = lane >> 5;
  c.voffX = (32u * w + r32) * 1024u + hh * 16u;
  const unsigned q4 = (lane >> 2) & 3u, p4 = lane & 3u, dhalf = (lane >> 4) & 1u;
  c.lbK = ldsb + r32 * 256u + ((hh ^ (r32 & 15u)) << 4);
  c.lbV = ldsb + (4u * hh + q4) * 256u + (q4 << 6) + 32u * dhalf + 8u * p4;
  const unsigned psw = ((p4 & 1u) << 1) | (p4 >> 1);
  c.lbW = ldsb + (8u * hh + q4) * 256u + (q4 << 6) + 32u * dhalf + 8u * psw;
  c.m0w = ldsb + w * 4096u;
  c.is = 0; c.cs = 0;
  constexpr unsigned WSTEP = 64u * 8192u, OOB = 0x7f000000u, KT = 16384u;

  bf16x8 x0[4], x1[4], x2[4], x3[4];
  bf16x8 pre[4];
  tile_all<TW>(c, 0);
  tile_all<TW>(c, WSTEP); xld_all(c, x0, b0 * (SQ * DM * 2));
  tile_all<TW>(c, 2u * WSTEP); xld_all(c, x1, b0 * (SQ * DM * 2) + 128u);
  tile_all<TW>(c, 3u * WSTEP); xld_all(c, x2, b0 * (SQ * DM * 2) + 256u);
  if (!typeB) __builtin_amdgcn_s_setprio(2);
  SYNC(24);
#pragma unroll
  for (unsigned i = 0; i < 4; ++i) pre[i] = wfrag(c.lbW, i);

  for (unsigned unit = 0; unit < 2; ++unit) {
    const unsigned b = b0 + 8u * unit;
    const unsigned xso = b * (SQ * DM * 2);
    const unsigned kv = (b * NH + h) * (SK * DH * 2);
    const unsigned xnx = unit == 0 ? xso + 8u * (SQ * DM * 2) : OOB;
    bf16x8 qf[8];
    {
      f32x16 acc[4] = {};
      if (unit == 0) WAITV(16);
      SYNCX(24, x0);
      qphase<TW, true, TW>(c, acc, x0, x3, pre, 4u * WSTEP, xso + 3u * 128u);
      if (unit == 0) WAITV(16);
      SYNCX(24, x1);
      qphase<TW, true, TW>(c, acc, x1, x0, pre, 5u * WSTEP, xso + 4u * 128u);
      if (unit == 0) WAITV(16);
      SYNCX(24, x2);
      qphase<TW, true, TW>(c, acc, x2, x1, pre, 6u * WSTEP, xso + 5u * 128u);
      SYNCX(16, x3);
      qphase<TW, true, TW>(c, acc, x3, x2, pre, 7u * WSTEP, xso + 6u * 128u);
      SYNCX(16, x0);
      qphase<TK, true, TW>(c, acc, x0, x3, pre, kv, xso + 7u * 128u);
      SYNCX(16, x1);
      qphase<TK, false, TW>(c, acc, x1, x0, pre, kv + KT, 0);
      SYNCX(12, x2);
      qphase<TV, false, TW>(c, acc, x2, x0, pre, kv, 0);
      SYNCX(8, x3);
      qphase<TK, false, TK>(c, acc, x3, x0, pre, kv + 2u * KT, 0);
#pragma unroll
      for (int db = 0; db < 4; ++db) { qf[2 * db] = pack8n(acc[db], 0); qf[2 * db + 1] = pack8n(acc[db], 8); }
    }
    __builtin_amdgcn_s_setprio(0);
    f32x16 o[4] = {};
    f32x16 sA0, sA1, sB0, sB1;
    u32x4 pw[4];
    float m, l = 0.f, mc;
    SYNC(8);
    aphase<false, TV, false>(c, qf, sA0, sA1, sB0, sB1, pw, pre, 0.f, l, kv + KT, x0, 0);
    {
      float alpha; bool resc;
      sm_max<true>(sA0, sA1, m, l, mc, alpha, resc);
      float ps0 = 0.f, ps1 = 0.f;
#pragma unroll
      for (int r = 0; r < 16; r += 2) { EXP2(sA0, r); EXP2(sA0, r + 1); ps0 += sA0[r]; ps1 += sA0[r + 1]; }
      l += ps0 + ps1;
    }
    SYNC(8);
    aphase<true, TK, false>(c, qf, sB0, sB1, sA0, sA1, pw, pre, mc, l, kv + 3u * KT, x0, 0);
    SYNC(8);
    bphase<false, TK, TV, false>(c, o, sB0, sB1, pw, pre, m, l, mc, kv + 2u * KT, x0, 0);
    for (unsigned t = 2; t < 6; t += 2) {
      SYNC(8);
      aphase<true, TK, false>(c, qf, sA0, sA1, sB0, sB1, pw, pre, mc, l, kv + (t + 2u) * KT, x0, 0);
      SYNC(8);
      bphase<false, TK, TV, false>(c, o, sA0, sA1, pw, pre, m, l, mc, kv + (t + 1u) * KT, x0, 0);
      SYNC(8);
      aphase<true, TK, false>(c, qf, sB0, sB1, sA0, sA1, pw, pre, mc, l, kv + (t + 3u) * KT, x0, 0);
      SYNC(8);
      bphase<false, TK, TV, false>(c, o, sB0, sB1, pw, pre, m, l, mc, kv + (t + 2u) * KT, x0, 0);
    }
    unsigned wn0 = 0u, wnr = 0u;
    if (unit == 1) {
      c.rw = __builtin_amdgcn_make_buffer_rsrc((void*)(br ? woB : woA), 0, NH * DH * DM * 2, 0x00020000);
      const unsigned j = (xcd & 3u) * 64u + idx;
      wn0 = (j >> 5) * 524288u + (j & 31u) * 256u; wnr = OOB;
    }
    SYNC(8);
    aphase<true, TV, false>(c, qf, sA0, sA1, sB0, sB1, pw, pre, mc, l, kv + 7u * KT, x0, 0);
    SYNC(8);
    bphase<false, TK, TW, false>(c, o, sA0, sA1, pw, pre, m, l, mc, wn0, x0, 0);
    SYNC(8);
    aphase<true, TW, true>(c, qf, sB0, sB1, sA0, sA1, pw, pre, mc, l, wnr + WSTEP, x0, xnx);
    SYNC(12);
    bphase<false, TV, TW, true>(c, o, sB0, sB1, pw, pre, m, l, mc, wnr + 2u * WSTEP, x1, xnx + 128u);
    SYNC(16);
    bphase<true, TW, TW, true>(c, o, sB0, sB1, pw, pre, m, l, mc, wnr + 3u * WSTEP, x2, xnx + 256u);
    const float inv = 1.0f / xhalf_sum(l);
    const unsigned u = (br * NB + b) * NH + h;
    bf16x8* op = (bf16x8*)ows + ((size_t)(u * 4u + w) * 8u) * 64u + lane;
#pragma unroll
    for (int db = 0; db < 4; ++db) {
      op[(2 * db) * 64] = pack8(o[db], 0, inv);
      op[(2 * db + 1) * 64] = pack8(o[db], 8, inv);
    }
    asm volatile("" ::: "memory");
  }
  WAITV(0);
}

template <int IMM> DI void ldo(bf16x8& d, unsigned voff, const void* base) {
  asm volatile("global_load_dwordx4 %0, %1, %2 offset:%3" : "=v"(d) : "v"(voff), "s"(base), "i"(IMM) : "memory");
}
DI void ldo_rt(bf16x8 (&S)[8], int i, unsigned vo, unsigned vo2, const void* base) {
  if (i == 0) ldo<0>(S[0], vo, base);
  if (i == 1) ldo<1024>(S[1], vo, base);
  if (i == 2) ldo<2048>(S[2], vo, base);
  if (i == 3) ldo<3072>(S[3], vo, base);
  if (i == 4) ldo<0>(S[4], vo2, base);
  if (i == 5) ldo<1024>(S[5], vo2, base);
  if (i == 6) ldo<2048>(S[6], vo2, base);
  if (i == 7) ldo<3072>(S[7], vo2, base);
}
#define WAITO(n, S)                                                                       \
  asm volatile("s_waitcnt vmcnt(" #n ") lgkmcnt(0)\n\ts_barrier"                          \
               : "+v"(S[0]), "+v"(S[1]), "+v"(S[2]), "+v"(S[3]), "+v"(S[4]), "+v"(S[5]),  \
                 "+v"(S[6]), "+v"(S[7])::"memory")
DI void wo_piece(int i, unsigned m0v, unsigned v, rsrc_t r, unsigned so) {
  if (i == 0) dma1<0, 0>(m0v, v, r, so);
  if (i == 1) dma1<1, 0>(m0v, v + 7168u, r, so);
  if (i == 2) dma1<2, 0>(m0v, v + 14336u, r, so);
  if (i == 3) dma1<3, 0>(m0v, v + 21504u, r, so);
}
template <bool ISSUE>
DI void ostep(f32x16& acc0, f32x16& acc1, const bf16x8 (&SC)[8], bf16x8 (&SN)[8], unsigned a0, unsigned m0v, unsigned voffWo,
              rsrc_t rwo, unsigned so, unsigned vo, unsigned vo2, const char* onext) {
  bf16x8 f[16];
  const unsigned a1 = a0 ^ 64u;
#define OFRAG(i) lds_tr2((((i) & 1) ? a1 : a0) + 2048u * ((i) >> 1), (((i) & 1) ? a1 : a0) + 2048u * ((i) >> 1) + 1024u)
  f[0] = OFRAG(0); f[1] = OFRAG(1); f[2] = OFRAG(2); f[3] = OFRAG(3);
  SB();
#pragma unroll
  for (unsigned i = 0; i < 16; i += 2) {
    if (i + 4 < 16) { f[i + 4] = OFRAG(i + 4); f[i + 5] = OFRAG(i + 5); }
    SB();
    acc0 = MFMA(f[i], SC[i >> 1], acc0);
    SB();
    if (ISSUE) { if (i < 8) wo_piece(i >> 1, m0v, voffWo, rwo, so); }
    SB();
    acc1 = MFMA(f[i + 1], SC[i >> 1], acc1);
    SB();
    if (ISSUE) ldo_rt(SN, i >> 1, vo, vo2, onext);
    SB();
  }
#undef OFRAG
}

__global__ __launch_bounds__(512, 2) void oproj_kernel(const __bf16* __restrict__ ows, const __bf16* __restrict__ woA,
                                                       const __bf16* __restrict__ woB, __bf16* __restrict__ out) {
  extern __shared__ __attribute__((aligned(16))) char smem[];
  const unsigned ldsb = (unsigned)(size_t)(LAS char*)smem;
  const unsigned tid = threadIdx.x, lane = tid & 63u;
  const unsigned w8 = __builtin_amdgcn_readfirstlane(tid >> 6);
  const unsigned w = w8 & 3u, g = w8 >> 2;
  const unsigned bid = blockIdx.x;
  const unsigned xcd = bid & 7u, idx = bid >> 3;
  const unsigned pair = xcd * 4u + (idx >> 3), nt = idx & 7u;
  const unsigned br = pair >> 4, b = pair & 15u;
  const __bf16* wo = (br ? woB : woA) + nt * 64u;
  const rsrc_t rwo = __builtin_amdgcn_make_buffer_rsrc((void*)wo, 0, ((NH * DH - 1) * DM + 64) * 2, 0x00020000);
  const unsigned voffWo = (32u * w + (lane >> 3)) * 1024u + (((lane & 7u) ^ (((lane >> 4) & 1u) << 2)) << 4);
  const unsigned hh = lane >> 5, q4 = (lane >> 2) & 3u, p4 = lane & 3u, dhalf = (lane >> 4) & 1u;
  const unsigned coloff = 16u * (p4 & 1u) + 8u * dhalf + 4u * (p4 >> 1);
  const unsigned ring = ldsb + g * (4u * SLOT);
  const unsigned lbA = ring + (4u * hh + q4) * 128u + (((q4 >> 1) & 1u) << 6) + 2u * coloff;
  const unsigned m0w = ring + w * 4096u;
  const char* obase = (const char*)ows + ((size_t)(pair * NH + 16u * g) * 4u + w) * 8192u;
  const unsigned wso = 16u * g * 131072u;
  const unsigned vo = lane * 16u, vo2 = lane * 16u + 4096u;

  bf16x8 B0[8], B1[8], B2[8], B3[8];
  f32x16 acc0 = {}, acc1 = {};
#pragma unroll
  for (int i = 0; i < 4; ++i) wo_piece(i, m0w, voffWo, rwo, wso);
#pragma unroll
  for (int i = 0; i < 8; ++i) ldo_rt(B0, i, vo, vo2, obase);
#pragma unroll
  for (int i = 0; i < 4; ++i) wo_piece(i, m0w + SLOT, voffWo, rwo, wso + 131072u);
#pragma unroll
  for (int i = 0; i < 8; ++i) ldo_rt(B1, i, vo, vo2, obase + 32768);
#pragma unroll
  for (int i = 0; i < 4; ++i) wo_piece(i, m0w + 2 * SLOT, voffWo, rwo, wso + 2u * 131072u);
#pragma unroll
  for (int i = 0; i < 8; ++i) ldo_rt(B2, i, vo, vo2, obase + 2 * 32768);

#define STEP(ISS, hd, SC, SN, WN)                                                          \
  do {                                                                                     \
    WAITO(WN, SC);                                                                         \
    ostep<ISS>(acc0, acc1, SC, SN, lbA + ((hd) & 3u) * SLOT, m0w + (((hd) + 3u) & 3u) * SLOT, voffWo, rwo, \
               wso + ((hd) + 3u) * 131072u, vo, vo2, obase + (size_t)((hd) + 3u) * 32768u); \
  } while (0)
  for (unsigned hd = 0; hd < 12; hd += 4) {
    STEP(true, hd, B0, B3, 24);
    STEP(true, hd + 1, B1, B0, 24);
    STEP(true, hd + 2, B2, B1, 24);
    STEP(true, hd + 3, B3, B2, 24);
  }
  STEP(true, 12u, B0, B3, 24);
  STEP(false, 13u, B1, B0, 24);
  STEP(false, 14u, B2, B1, 12);
  STEP(false, 15u, B3, B2, 0);
#undef STEP
  BARRIER();
  typedef __attribute__((ext_vector_type(4))) float f32x4;
  LAS f32x4* red = (LAS f32x4*)(ldsb + w * 8192u) + lane;
  if (g == 1) {
#pragma unroll
    for (int i = 0; i < 4; ++i) {
      red[i * 64] = f32x4{acc0[4 * i], acc0[4 * i + 1], acc0[4 * i + 2], acc0[4 * i + 3]};
      red[(4 + i) * 64] = f32x4{acc1[4 * i], acc1[4 * i + 1], acc1[4 * i + 2], acc1[4 * i + 3]};
    }
  }
  BARRIER();
  if (g == 0) {
#pragma unroll
    for (int i = 0; i < 4; ++i) {
      f32x4 a = red[i * 64], c2 = red[(4 + i) * 64];
#pragma unroll
      for (int e = 0; e < 4; ++e) { acc0[4 * i + e] += a[e]; acc1[4 * i + e] += c2[e]; }
    }
    const unsigned r32 = lane & 31u;
    __bf16* orow = out + ((size_t)b * SQ + 32u * w + r32) * 1024u + br * 512u + nt * 64u + 16u * hh;
    *(bf16x8*)(orow) = pack8n(acc0, 0);
    *(bf16x8*)(orow + 8) = pack8n(acc0, 8);
    *(bf16x8*)(orow + 32) = pack8n(acc1, 0);
    *(bf16x8*)(orow + 40) = pack8n(acc1, 8);
  }
}

extern "C" void kernel_launch(void* const* d_in, const int* in_sizes, int n_in, void* d_out, int out_size, void* d_ws,
                              size_t ws_size, hipStream_t stream) {
  const __bf16* xA = (const __bf16*)d_in[0];
  const __bf16* kA = (const __bf16*)d_in[1];
  const __bf16* vA = (const __bf16*)d_in[2];
  const __bf16* wqA = (const __bf16*)d_in[3];
  const __bf16* woA = (const __bf16*)d_in[4];
  const __bf16* xB = (const __bf16*)d_in[5];
  const __bf16* kB = (const __bf16*)d_in[6];
  const __bf16* vB = (const __bf16*)d_in[7];
  const __bf16* wqB = (const __bf16*)d_in[8];
  const __bf16* woB = (const __bf16*)d_in[9];
  __bf16* ows = (__bf16*)d_ws;
  hipFuncSetAttribute((const void*)attn_kernel, hipFuncAttributeMaxDynamicSharedMemorySize, NSLOT * SLOT);
  hipFuncSetAttribute((const void*)oproj_kernel, hipFuncAttributeMaxDynamicSharedMemorySize, 8 * SLOT);
  hipLaunchKernelGGL(attn_kernel, dim3(512), dim3(256), NSLOT * SLOT, stream, xA, kA, vA, wqA, woA, xB, kB, vB, wqB, woB, ows);
  hipLaunchKernelGGL(oproj_kernel, dim3(256), dim3(512), 8 * SLOT, stream, (const __bf16*)ows, woA, woB, (__bf16*)d_out);
}
```

```cpp
#include <hip/hip_runtime.h>
#include <hip/hip_bf16.h>
#include <stdint.h>

typedef __attribute__((ext_vector_type(8))) __bf16 bf16x8;
typedef __attribute__((ext_vector_type(4))) __bf16 bf16x4;
typedef __attribute__((ext_vector_type(2))) __bf16 bf16x2;
typedef __attribute__((ext_vector_type(16))) float f32x16;
typedef __attribute__((ext_vector_type(2))) float f32x2;
typedef __attribute__((ext_vector_type(4))) unsigned u32x4;
#define LAS __attribute__((address_space(3)))
#define DI __device__ __forceinline__
#define AI __attribute__((always_inline))
typedef __amdgpu_buffer_rsrc_t rsrc_t;

constexpr int NB = 16, SQ = 128, DM = 512, NH = 32, SK = 512, DH = 128;
constexpr unsigned SLOT = 16384, NSLOT = 5;

#define WAITV(n) asm volatile("s_waitcnt vmcnt(" #n ")" ::: "memory")
#define BARRIER() asm volatile("s_waitcnt lgkmcnt(0)\n\ts_barrier" ::: "memory")

DI void dma16(unsigned m0v, unsigned voff, rsrc_t r, unsigned soff) {
  asm volatile("s_mov_b32 m0, %0\n\ts_nop 0\n\tbuffer_load_dwordx4 %1, %2, %3 offen lds"
               :: "s"(m0v), "v"(voff), "s"(r), "s"(soff) : "memory");
}
DI void dma16nt(unsigned m0v, unsigned voff, rsrc_t r, unsigned soff) {
  asm volatile("s_mov_b32 m0, %0\n\ts_nop 0\n\tbuffer_load_dwordx4 %1, %2, %3 offen nt lds"
               :: "s"(m0v), "v"(voff), "s"(r), "s"(soff) : "memory");
}
DI unsigned cvtpk(float lo, float hi) {
  f32x2 v = {lo, hi};
  bf16x2 b = __builtin_convertvector(v, bf16x2);
  return __builtin_bit_cast(unsigned, b);
}
DI bf16x8 pack8(const f32x16& a, int base, float sc) {
  u32x4 w = {cvtpk(a[base + 0] * sc, a[base + 1] * sc), cvtpk(a[base + 2] * sc, a[base + 3] * sc),
             cvtpk(a[base + 4] * sc, a[base + 5] * sc), cvtpk(a[base + 6] * sc, a[base + 7] * sc)};
  return __builtin_bit_cast(bf16x8, w);
}
DI bf16x8 pack8n(const f32x16& a, int base) {
  u32x4 w = {cvtpk(a[base + 0], a[base + 1]), cvtpk(a[base + 2], a[base + 3]),
             cvtpk(a[base + 4], a[base + 5]), cvtpk(a[base + 6], a[base + 7])};
  return __builtin_bit_cast(bf16x8, w);
}
DI bf16x8 lds_b128(unsigned addr) { return *(LAS const bf16x8*)(addr); }
DI bf16x8 lds_tr2(unsigned addr0, unsigned addr1) {
  bf16x4 lo = __builtin_amdgcn_ds_read_tr16_b64_v4bf16((LAS bf16x4*)(addr0));
  bf16x4 hi = __builtin_amdgcn_ds_read_tr16_b64_v4bf16((LAS bf16x4*)(addr1));
  return __builtin_shufflevector(lo, hi, 0, 1, 2, 3, 4, 5, 6, 7);
}
DI float xhalf_max(float x) {
  auto r = __builtin_amdgcn_permlane32_swap(__float_as_uint(x), __float_as_uint(x), false, false);
  return fmaxf(__uint_as_float(r[0]), __uint_as_float(r[1]));
}
DI float xhalf_sum(float x) {
  auto r = __builtin_amdgcn_permlane32_swap(__float_as_uint(x), __float_as_uint(x), false, false);
  return __uint_as_float(r[0]) + __uint_as_float(r[1]);
}
#define MFMA(a, b, c) __builtin_amdgcn_mfma_f32_32x32x16_bf16((a), (b), (c), 0, 0, 0)

#define SB() __builtin_amdgcn_sched_barrier(0)
DI float max3f(float a, float b, float c) { float r; asm("v_max3_f32 %0, %1, %2, %3" : "=v"(r) : "v"(a), "v"(b), "v"(c)); return r; }
DI unsigned cvtpk_pin(float lo, float hi) { unsigned t = cvtpk(lo, hi); asm volatile("" : "+v"(t)); return t; }
#define PIN(x) asm volatile("" : "+v"(x))
#define PIN2(x, y) asm volatile("" : "+v"(x), "+v"(y))
#define ADD0(acc, x) acc += (x)
#define ADD1(acc, x) acc = __builtin_fmaf((x), one, acc)
template <int I, int NT> DI void dma1(unsigned m0v, unsigned v, rsrc_t r, unsigned soff) {
  if (NT) asm volatile("s_mov_b32 m0, %0\n\ts_nop 0\n\tbuffer_load_dwordx4 %1, %2, %3 offen offset:%4 nt lds"
                       :: "s"(m0v), "v"(v), "s"(r), "s"(soff), "i"(I * 1024) : "memory");
  else asm volatile("s_mov_b32 m0, %0\n\ts_nop 0\n\tbuffer_load_dwordx4 %1, %2, %3 offen offset:%4 lds"
                    :: "s"(m0v), "v"(v), "s"(r), "s"(soff), "i"(I * 1024) : "memory");
}
DI void xld(bf16x8& d, unsigned v, rsrc_t r, unsigned soff) {
  asm volatile("buffer_load_dwordx4 %0, %1, %2, %3 offen" : "=v"(d) : "v"(v), "s"(r), "s"(soff) : "memory");
}
enum { TW = 1, TK = 2, TV = 3 };

struct Ctx {
  rsrc_t rx, rw, rk, rv;
  unsigned voffK, voffV, voffW, voffX, lbK, lbV, lbW, lbX, lwX, m0w;
  float one;
};
template <int T, int I, int SL> DI void piece(const Ctx& c, unsigned so) {
  if (T == TW) dma1<I, 0>(c.m0w + SL * SLOT, c.voffW + I * 31744u, c.rw, so);
  if (T == TK) dma1<I, 1>(c.m0w + SL * SLOT, c.voffK ^ (I << 6), c.rk, so);
  if (T == TV) dma1<I, 1>(c.m0w + SL * SLOT, c.voffV, c.rv, so);
}
template <int T, int SL> DI void piece_rt(const Ctx& c, int i, unsigned so) {
  if (i == 0) piece<T, 0, SL>(c, so);
  if (i == 1) piece<T, 1, SL>(c, so);
  if (i == 2) piece<T, 2, SL>(c, so);
  if (i == 3) piece<T, 3, SL>(c, so);
}
template <int T, int SL> DI void tile_all(const Ctx& c, unsigned so) {
  piece<T, 0, SL>(c, so); piece<T, 1, SL>(c, so); piece<T, 2, SL>(c, so); piece<T, 3, SL>(c, so);
}
DI void xld1(const Ctx& c, bf16x8 (&x)[4], int i, unsigned so) { xld(x[i], c.voffX, c.rx, so + i * 8192u); }
DI void xld_all(const Ctx& c, bf16x8 (&x)[4], unsigned so) { xld1(c, x, 0, so); xld1(c, x, 1, so); xld1(c, x, 2, so); xld1(c, x, 3, so); }
DI void xwr(const Ctx& c, const bf16x8 (&x)[4]) {
#pragma unroll
  for (unsigned i = 0; i < 4; ++i) *(LAS bf16x8*)((c.lwX ^ ((i & 1u) << 6)) + i * 1024u) = x[i];
}
DI bf16x8 xfrag(const Ctx& c, unsigned ks) { return lds_b128(c.lbX ^ (ks << 5)); }
#define TIE4(X) "+v"(X[0]), "+v"(X[1]), "+v"(X[2]), "+v"(X[3])
#define SYNCX(n, X) asm volatile("s_waitcnt vmcnt(" #n ") lgkmcnt(0)\n\ts_barrier" : TIE4(X) :: "memory")
#define SYNCX2(n, X, Y) asm volatile("s_waitcnt vmcnt(" #n ") lgkmcnt(0)\n\ts_barrier" : TIE4(X), TIE4(Y) :: "memory")
#define SYNC(n) asm volatile("s_waitcnt vmcnt(" #n ") lgkmcnt(0)\n\ts_barrier" ::: "memory")

constexpr float SMC = 0.08838834764831845f * 1.4426950408889634f;
constexpr float THR = 8.0f;
#define EXP2(S, R) S[R] = __builtin_amdgcn_exp2f(S[R])
template <int T, int SL> DI bf16x8 frag(const Ctx& c, unsigned i) {
  if (T == TK) return lds_b128((c.lbK ^ ((i >> 1) << 5)) + 8192u * (i & 1u) + SL * SLOT);
  if (T == TV) { const unsigned a = (c.lbV ^ ((i & 3u) << 6)) + 4096u * (i >> 2) + SL * SLOT; return lds_tr2(a, a + 2048u); }
  const unsigned a = (c.lbW ^ ((i & 3u) << 6)) + 4096u * (i >> 2) + SL * SLOT; return lds_tr2(a, a + 1024u);
}

template <int P, int T0, bool XW, bool XL, int TN>
DI void qphase(const Ctx& c, f32x16 (&acc)[4], const bf16x8 (&xb)[4], bf16x8 (&xn)[4], const bf16x8 (&xw)[4], bf16x8 (&xl)[4],
               bf16x8 (&pre)[4], unsigned so0, unsigned xso) {
  constexpr int SL = P & 3, SN = (P + 1) & 3, SI = (P + 3) & 3;
  bf16x8 af[16];
  af[0] = pre[0]; af[1] = pre[1]; af[2] = pre[2]; af[3] = pre[3];
  SB();
#pragma unroll
  for (unsigned i = 0; i < 16; i += 2) {
    if (i + 4 < 16) { af[i + 4] = frag<TW, SL>(c, i + 4); af[i + 5] = frag<TW, SL>(c, i + 5); }
    else { pre[i - 12] = frag<TN, SN>(c, i - 12); pre[i - 11] = frag<TN, SN>(c, i - 11); }
    if (XW && i == 2) xwr(c, xw);
    if (XW && i == 8) { xn[0] = xfrag(c, 0); xn[1] = xfrag(c, 1); }
    if (XW && i == 10) { xn[2] = xfrag(c, 2); xn[3] = xfrag(c, 3); }
    SB();
    acc[i & 3] = MFMA(af[i], xb[i >> 2], acc[i & 3]);
    SB();
    if (i < 8) piece_rt<T0, SI>(c, i >> 1, so0);
    else if (XL) xld1(c, xl, (i - 8) >> 1, xso);
    SB();
    acc[(i + 1) & 3] = MFMA(af[i + 1], xb[i >> 2], acc[(i + 1) & 3]);
    SB();
  }
}

template <int P, bool FIN, int T0>
DI void aphase(const Ctx& c, const bf16x8 (&qf)[8], const f32x16& negm, f32x16& n0, f32x16& n1, f32x16& p0, f32x16& p1,
               u32x4 (&pw)[4], bf16x8 (&pre)[4], float& l, unsigned so0) {
  constexpr int SL = P & 3, SN = (P + 1) & 3, SI = (P + 3) & 3;
  const float one = c.one;
  bf16x8 kf[16];
  kf[0] = pre[0]; kf[1] = pre[1]; kf[2] = pre[2]; kf[3] = pre[3];
  float ps0 = l, ps1 = 0.f;
  SB();
#pragma unroll
  for (unsigned i = 0; i < 16; i += 2) {
    if (i + 4 < 16) { kf[i + 4] = frag<TK, SL>(c, i + 4); kf[i + 5] = frag<TK, SL>(c, i + 5); }
    else { pre[i - 12] = frag<(FIN ? TV : TK), SN>(c, i - 12); pre[i - 11] = frag<(FIN ? TV : TK), SN>(c, i - 11); }
    SB();
    if (i == 0) { if (FIN) n0 = MFMA(kf[i], qf[i >> 1], negm); else { f32x16 z = {}; n0 = MFMA(kf[i], qf[i >> 1], z); } }
    else n0 = MFMA(kf[i], qf[i >> 1], n0);
    SB();
    if (FIN) {
      EXP2(p1, i); EXP2(p1, i + 1);
      pw[i >> 3][(i >> 1) & 3] = cvtpk_pin(p0[i], p0[i + 1]);
    }
    if (i < 8) piece_rt<T0, SI>(c, i >> 1, so0);
    SB();
    if (i == 0) { if (FIN) n1 = MFMA(kf[i + 1], qf[i >> 1], negm); else { f32x16 z = {}; n1 = MFMA(kf[i + 1], qf[i >> 1], z); } }
    else n1 = MFMA(kf[i + 1], qf[i >> 1], n1);
    SB();
    if (FIN) {
      pw[2 + (i >> 3)][(i >> 1) & 3] = cvtpk_pin(p1[i], p1[i + 1]);
      ADD0(ps0, p1[i]);
      if (i == 0) ps1 = p1[1]; else ADD1(ps1, p1[i + 1]);
      PIN2(ps0, ps1);
    }
    SB();
  }
  if (FIN) l = ps0 + ps1;
}

DI float row_max(f32x16& s0, f32x16& s1) {
  float a, b;
  asm("v_max3_f32 %0, %2, %3, %4\n\tv_max3_f32 %1, %15, %16, %17\n\t"
      "v_max3_f32 %0, %0, %5, %6\n\tv_max3_f32 %1, %1, %18, %19\n\t"
      "v_max3_f32 %0, %0, %7, %8\n\tv_max3_f32 %1, %1, %20, %21\n\t"
      "v_max3_f32 %0, %0, %9, %10\n\tv_max3_f32 %1, %1, %22, %23\n\t"
      "v_max3_f32 %0, %0, %11, %12\n\tv_max3_f32 %1, %1, %24, %25\n\t"
      "v_max3_f32 %0, %0, %13, %14\n\tv_max3_f32 %1, %1, %26, %27"
      : "=&v"(a), "=&v"(b)
      : "v"(s0[0]), "v"(s0[1]), "v"(s0[2]), "v"(s0[3]), "v"(s0[4]), "v"(s0[5]), "v"(s0[6]), "v"(s0[7]), "v"(s0[8]), "v"(s0[9]),
        "v"(s0[10]), "v"(s0[11]), "v"(s0[12]),
        "v"(s1[0]), "v"(s1[1]), "v"(s1[2]), "v"(s1[3]), "v"(s1[4]), "v"(s1[5]), "v"(s1[6]), "v"(s1[7]), "v"(s1[8]), "v"(s1[9]),
        "v"(s1[10]), "v"(s1[11]), "v"(s1[12]));
  asm("v_max3_f32 %0, %0, %2, %3\n\tv_max3_f32 %1, %1, %4, %5\n\t"
      "v_max3_f32 %0, %0, %6, %7\n\tv_max_f32_e32 %0, %0, %1\n\t"
      "v_mov_b32_e32 %1, %0\n\ts_nop 1\n\tv_permlane32_swap_b32_e32 %0, %1\n\tv_max_f32_e32 %0, %0, %1"
      : "+v"(a), "+v"(b)
      : "v"(s0[13]), "v"(s0[14]), "v"(s1[13]), "v"(s1[14]), "v"(s0[15]), "v"(s1[15]));
  return a;
}

template <int P, bool LAST, int TN, int T0, int XL>
DI void bphase(const Ctx& c, f32x16 (&o)[4], f32x16& negm, f32x16& s0, f32x16& s1, u32x4 (&pw)[4], bf16x8 (&pre)[4], float& l,
               unsigned so0, bf16x8 (&xa)[4], bf16x8 (&xb)[4], unsigned xso) {
  constexpr int SL = P & 3, SN = (P + 1) & 3, SI = (P + 3) & 3;
  const float one = c.one;
  bf16x8 vf[16];
  vf[0] = pre[0]; vf[1] = pre[1]; vf[2] = pre[2]; vf[3] = pre[3];
  float ps0 = 0.f, ps1 = 0.f, alpha = 1.f;
  bool resc = false;
  if (LAST) {
#pragma unroll
    for (int e = 0; e < 8; ++e) pw[e >> 2][e & 3] = cvtpk(s0[2 * e], s0[2 * e + 1]);
  }
  SB();
#pragma unroll
  for (unsigned i = 0; i < 16; i += 2) {
    const unsigned ks = i >> 2, db = i & 3u;
    if (i + 4 < 16) { vf[i + 4] = frag<TV, SL>(c, i + 4); vf[i + 5] = frag<TV, SL>(c, i + 5); }
    else { pre[i - 12] = frag<TN, SN>(c, i - 12); pre[i - 11] = frag<TN, SN>(c, i - 11); }
    SB();
    o[db] = MFMA(vf[i], __builtin_bit_cast(bf16x8, pw[ks]), o[db]);
    SB();
    if (i < 8) piece_rt<T0, SI>(c, i >> 1, so0);
    else if (XL >= 1) xld1(c, xa, (i - 8) >> 1, xso);
    if (LAST) {
      if (i < 8) {
        EXP2(s1, 2 * i); EXP2(s1, 2 * i + 1);
        pw[2 + (i >> 2)][i & 3] = cvtpk(s1[2 * i], s1[2 * i + 1]);
        if (i == 0) { ps0 = s1[0]; ps1 = s1[1]; } else { ADD0(ps0, s1[2 * i]); ADD1(ps1, s1[2 * i + 1]); }
        PIN2(ps0, ps1);
      }
    } else {
      if (i == 2) {
        const float rmax = row_max(s0, s1);
        if (__builtin_expect(__any(rmax > THR), 0)) {
          const float dl = fmaxf(rmax, 0.f);
          alpha = __builtin_amdgcn_exp2f(-dl);
          l *= alpha; resc = true;
#pragma unroll
          for (int r = 0; r < 16; ++r) { s0[r] -= dl; s1[r] -= dl; }
          asm volatile("v_sub_f32 %0, %0, %16\n\tv_sub_f32 %1, %1, %16\n\tv_sub_f32 %2, %2, %16\n\tv_sub_f32 %3, %3, %16\n\t"
                       "v_sub_f32 %4, %4, %16\n\tv_sub_f32 %5, %5, %16\n\tv_sub_f32 %6, %6, %16\n\tv_sub_f32 %7, %7, %16\n\t"
                       "v_sub_f32 %8, %8, %16\n\tv_sub_f32 %9, %9, %16\n\tv_sub_f32 %10, %10, %16\n\tv_sub_f32 %11, %11, %16\n\t"
                       "v_sub_f32 %12, %12, %16\n\tv_sub_f32 %13, %13, %16\n\tv_sub_f32 %14, %14, %16\n\tv_sub_f32 %15, %15, %16\n\ts_nop 1"
                       : "+v"(negm[0]), "+v"(negm[1]), "+v"(negm[2]), "+v"(negm[3]), "+v"(negm[4]), "+v"(negm[5]), "+v"(negm[6]), "+v"(negm[7]),
                         "+v"(negm[8]), "+v"(negm[9]), "+v"(negm[10]), "+v"(negm[11]), "+v"(negm[12]), "+v"(negm[13]), "+v"(negm[14]), "+v"(negm[15])
                       : "v"(dl));
        }
      }
      if (i >= 6) {
        EXP2(s0, (i - 6) / 2 * 3); EXP2(s0, (i - 6) / 2 * 3 + 1);
        if (i == 6) ps0 = s0[0]; else ADD0(ps0, s0[(i - 6) / 2 * 3]);
        ADD1(ps1, s0[(i - 6) / 2 * 3 + 1]);
        PIN2(ps0, ps1); PIN2(s0[(i - 6) / 2 * 3], s0[(i - 6) / 2 * 3 + 1]);
      }
    }
    SB();
    o[db + 1] = MFMA(vf[i + 1], __builtin_bit_cast(bf16x8, pw[ks]), o[db + 1]);
    SB();
    if (i >= 8 && XL >= 2) xld1(c, xb, (i - 8) >> 1, xso + 128u);
    if (LAST) {
      if (i < 8) {
        EXP2(s1, 2 * i + 2); EXP2(s1, 2 * i + 3);
        pw[2 + (i >> 2)][(i & 3) + 1] = cvtpk(s1[2 * i + 2], s1[2 * i + 3]);
        ADD0(ps0, s1[2 * i + 2]); ADD1(ps1, s1[2 * i + 3]);
        PIN2(ps0, ps1);
      }
    } else {
      if (i >= 6) { EXP2(s0, (i - 6) / 2 * 3 + 2); ADD0(ps0, s0[(i - 6) / 2 * 3 + 2]); PIN2(ps0, s0[(i - 6) / 2 * 3 + 2]); }
      if (i == 4) { EXP2(s0, 15); PIN(s0[15]); ps1 = s0[15]; }
    }
    SB();
  }
  l += ps0 + ps1;
  if (!LAST) {
    if (__builtin_expect(resc, 0)) {
#pragma unroll
      for (int db = 0; db < 4; ++db)
#pragma unroll
        for (int r = 0; r < 16; ++r) o[db][r] *= alpha;
    }
  }
}

__global__ __launch_bounds__(256, 2) void attn_kernel(
    const __bf16* __restrict__ xA, const __bf16* __restrict__ kA, const __bf16* __restrict__ vA, const __bf16* __restrict__ wqA,
    const __bf16* __restrict__ woA,
    const __bf16* __restrict__ xB, const __bf16* __restrict__ kB, const __bf16* __restrict__ vB, const __bf16* __restrict__ wqB,
    const __bf16* __restrict__ woB, __bf16* __restrict__ ows) {
  extern __shared__ __attribute__((aligned(16))) char smem[];
  const unsigned ldsb = (unsigned)(size_t)(LAS char*)smem;
  const unsigned tid = threadIdx.x, lane = tid & 63u;
  const unsigned w = __builtin_amdgcn_readfirstlane(tid >> 6);
  const unsigned bid = blockIdx.x;
  const unsigned typeB = (__builtin_amdgcn_s_getreg(6 | (11 << 11)) != 0) ? 1u : 0u;
  const unsigned xcd = bid & 7u, idx = bid >> 3;
  const unsigned br = xcd >> 2, h = (xcd & 3u) * 8u + (idx & 7u), b0 = idx >> 3;
  Ctx c;
  c.rx = __builtin_amdgcn_make_buffer_rsrc((void*)(br ? xB : xA), 0, NB * SQ * DM * 2, 0x00020000);
  c.rw = __builtin_amdgcn_make_buffer_rsrc((void*)((br ? wqB : wqA) + (size_t)h * DH), 0, ((DM - 1) * NH * DH + DH) * 2, 0x00020000);
  c.rk = __builtin_amdgcn_make_buffer_rsrc((void*)(br ? kB : kA), 0, NB * NH * SK * DH * 2, 0x00020000);
  c.rv = __builtin_amdgcn_make_buffer_rsrc((void*)(br ? vB : vA), 0, NB * NH * SK * DH * 2, 0x00020000);
  const unsigned l4 = lane >> 4, l15 = lane & 15u;
  c.voffK = (16u * w + l4) * 256u + ((l15 ^ l4) << 4);
  c.voffV = (16u * w + l4) * 256u + ((l15 ^ (l4 << 2)) << 4);
  c.voffW = (16u * w + l4) * 8192u + ((l15 ^ (l4 << 2)) << 4);
  c.voffX = (32u * w + (lane >> 3)) * 1024u + ((lane & 7u) << 4);
  const unsigned r32 = lane & 31u, hh = lane >> 5;
  const unsigned q4 = (lane >> 2) & 3u, p4 = lane & 3u, dhalf = (lane >> 4) & 1u;
  c.lbK = ldsb + r32 * 256u + ((hh ^ (r32 & 15u)) << 4);
  c.lbV = ldsb + (4u * hh + q4) * 256u + (q4 << 6) + 32u * dhalf + 8u * p4;
  const unsigned psw = ((p4 & 1u) << 1) | (p4 >> 1);
  c.lbW = ldsb + (8u * hh + q4) * 256u + (q4 << 6) + 32u * dhalf + 8u * psw;
  const unsigned xbuf = ldsb + 4u * SLOT + w * 4096u;
  c.lbX = xbuf + r32 * 128u + ((hh ^ ((r32 >> 1) & 7u)) << 4);
  c.lwX = xbuf + (lane >> 3) * 128u + (((lane & 7u) ^ l4) << 4);
  c.m0w = ldsb + w * 4096u;
  c.one = 1.0f;
  asm volatile("" : "+v"(c.one));
  const float one = c.one;
  constexpr unsigned WSTEP = 64u * 8192u, OOB = 0x7f000000u, KT = 16384u;

  bf16x8 r0[4], r1[4], r2[4];
  bf16x8 pre[4];
  tile_all<TW, 0>(c, 0);
  tile_all<TW, 1>(c, WSTEP); xld_all(c, r0, b0 * (SQ * DM * 2)); xld_all(c, r1, b0 * (SQ * DM * 2) + 128u);
  tile_all<TW, 2>(c, 2u * WSTEP); xld_all(c, r2, b0 * (SQ * DM * 2) + 256u);
  if (!typeB) __builtin_amdgcn_s_setprio(2);
  SYNC(20);
#pragma unroll
  for (unsigned i = 0; i < 4; ++i) pre[i] = frag<TW, 0>(c, i);

  for (unsigned unit = 0; unit < 2; ++unit) {
    const unsigned b = b0 + 8u * unit;
    const unsigned xso = b * (SQ * DM * 2);
    const unsigned kv = (b * NH + h) * (SK * DH * 2);
    const unsigned xnx = unit == 0 ? xso + 8u * (SQ * DM * 2) : OOB;
    const unsigned wnx = unit == 0 ? 0u : OOB;
    bf16x8 qf[8];
    {
      f32x16 acc[4] = {};
      bf16x8 fa[4], fb[4];
      asm volatile("" : "+v"(c.lbW), "+v"(c.lbX), "+v"(c.lwX), "+v"(c.voffW), "+v"(c.lbK), "+v"(c.voffK));
      if (unit == 0) WAITV(8);
      SYNCX2(16, r0, r1);
      xwr(c, r0);
      fa[0] = xfrag(c, 0); fa[1] = xfrag(c, 1); fa[2] = xfrag(c, 2); fa[3] = xfrag(c, 3);
      qphase<0, TW, true, true, TW>(c, acc, fa, fb, r1, r0, pre, 3u * WSTEP, xso + 3u * 128u);
      if (unit == 0) WAITV(8);
      SYNCX(16, r2);
      qphase<1, TW, true, true, TW>(c, acc, fb, fa, r2, r1, pre, 4u * WSTEP, xso + 4u * 128u);
      SYNCX(8, r0);
      qphase<2, TW, true, true, TW>(c, acc, fa, fb, r0, r2, pre, 5u * WSTEP, xso + 5u * 128u);
      SYNCX(8, r1);
      qphase<3, TW, true, true, TW>(c, acc, fb, fa, r1, r0, pre, 6u * WSTEP, xso + 6u * 128u);
      SYNCX(8, r2);
      qphase<4, TW, true, true, TW>(c, acc, fa, fb, r2, r1, pre, 7u * WSTEP, xso + 7u * 128u);
      SYNCX(8, r0);
      qphase<5, TK, true, false, TW>(c, acc, fb, fa, r0, r2, pre, kv, 0);
      SYNCX(4, r1);
      qphase<6, TK, true, false, TW>(c, acc, fa, fb, r1, r2, pre, kv + KT, 0);
      SYNC(4);
      qphase<7, TV, false, false, TK>(c, acc, fb, fa, r1, r2, pre, kv, 0);
#pragma unroll
      for (int db = 0; db < 4; ++db) { qf[2 * db] = pack8(acc[db], 0, SMC); qf[2 * db + 1] = pack8(acc[db], 8, SMC); }
    }
    __builtin_amdgcn_s_setprio(0);
    f32x16 o[4] = {};
    f32x16 sA0, sA1, sB0, sB1, negm;
    u32x4 pw[4];
    float l = 0.f;
    asm volatile("" : "+v"(c.lbK), "+v"(c.lbV), "+v"(c.voffK));
    SYNC(4);
    aphase<8, false, TK>(c, qf, negm, sA0, sA1, sB0, sB1, pw, pre, l, kv + 2u * KT);
    {
      asm volatile("s_nop 7\n\ts_nop 7" : "+v"(sA0), "+v"(sA1));
      const float m = row_max(sA0, sA1);
      float ps0 = 0.f, ps1 = 0.f;
#pragma unroll
      for (int r = 0; r < 16; ++r) { sA0[r] -= m; sA1[r] -= m; negm[r] = -m; }
      asm volatile("" : "+v"(negm));
#pragma unroll
      for (int r = 0; r < 16; r += 2) { EXP2(sA0, r); EXP2(sA0, r + 1); ADD0(ps0, sA0[r]); ADD1(ps1, sA0[r + 1]); }
      l = ps0 + ps1;
    }
    if (unit == 0) {
      const rsrc_t rwo = __builtin_amdgcn_make_buffer_rsrc((void*)(br ? woB : woA), 0, NH * DH * DM * 2, 0x00020000);
      asm volatile("s_mov_b32 m0, %0\n\ts_nop 0\n\tbuffer_load_dword %1, %2, %3 offen lds"
                   :: "s"(xbuf), "v"(r32 * 128u), "s"(rwo), "s"((((xcd & 3u) * 64u + idx) * 4u + w) * 4096u) : "memory");
    }
    SYNC(4);
    aphase<9, true, TV>(c, qf, negm, sB0, sB1, sA0, sA1, pw, pre, l, kv + KT);
    SYNC(4);
    bphase<10, false, TK, TK, 0>(c, o, negm, sB0, sB1, pw, pre, l, kv + 3u * KT, r0, r1, 0);
    for (unsigned t = 2; t < 6; t += 2) {
      SYNC(4);
      aphase<11, true, TV>(c, qf, negm, sA0, sA1, sB0, sB1, pw, pre, l, kv + t * KT);
      SYNC(4);
      bphase<12, false, TK, TK, 0>(c, o, negm, sA0, sA1, pw, pre, l, kv + (t + 2u) * KT, r0, r1, 0);
      SYNC(4);
      aphase<13, true, TV>(c, qf, negm, sB0, sB1, sA0, sA1, pw, pre, l, kv + (t + 1u) * KT);
      SYNC(4);
      bphase<14, false, TK, TK, 0>(c, o, negm, sB0, sB1, pw, pre, l, kv + (t + 3u) * KT, r0, r1, 0);
    }
    SYNC(4);
    aphase<19, true, TV>(c, qf, negm, sA0, sA1, sB0, sB1, pw, pre, l, kv + 6u * KT);
    SYNC(4);
    bphase<20, false, TK, TV, 0>(c, o, negm, sA0, sA1, pw, pre, l, kv + 7u * KT, r0, r1, 0);
    asm volatile("" : "+v"(c.lbW), "+v"(c.voffW));
    SYNC(4);
    aphase<21, true, TW>(c, qf, negm, sB0, sB1, sA0, sA1, pw, pre, l, wnx);
    SYNC(4);
    bphase<22, false, TV, TW, 2>(c, o, negm, sB0, sB1, pw, pre, l, wnx + WSTEP, r0, r1, xnx);
    SYNC(12);
    bphase<23, true, TW, TW, 1>(c, o, negm, sB0, sB1, pw, pre, l, wnx + 2u * WSTEP, r2, r2, xnx + 256u);
    const float inv = 1.0f / xhalf_sum(l);
    const unsigned u = (br * NB + b) * NH + h;
    bf16x8* op = (bf16x8*)ows + ((size_t)(u * 4u + w) * 8u) * 64u + lane;
#pragma unroll
    for (int db = 0; db < 4; ++db) {
      op[(2 * db) * 64] = pack8(o[db], 0, inv);
      op[(2 * db + 1) * 64] = pack8(o[db], 8, inv);
    }
    asm volatile("" ::: "memory");
  }
  WAITV(0);
}

template <int IMM> DI void ldo(bf16x8& d, unsigned voff, const void* base) {
  asm volatile("global_load_dwordx4 %0, %1, %2 offset:%3" : "=v"(d) : "v"(voff), "s"(base), "i"(IMM) : "memory");
}
DI void ldo_rt(bf16x8 (&S)[8], int i, unsigned vo, unsigned vo2, const void* base) {
  if (i == 0) ldo<0>(S[0], vo, base);
  if (i == 1) ldo<1024>(S[1], vo, base);
  if (i == 2) ldo<2048>(S[2], vo, base);
  if (i == 3) ldo<3072>(S[3], vo, base);
  if (i == 4) ldo<0>(S[4], vo2, base);
  if (i == 5) ldo<1024>(S[5], vo2, base);
  if (i == 6) ldo<2048>(S[6], vo2, base);
  if (i == 7) ldo<3072>(S[7], vo2, base);
}
#define WAITO(n, S)                                                                       \
  asm volatile("s_waitcnt vmcnt(" #n ") lgkmcnt(0)\n\ts_barrier"                          \
               : "+v"(S[0]), "+v"(S[1]), "+v"(S[2]), "+v"(S[3]), "+v"(S[4]), "+v"(S[5]),  \
                 "+v"(S[6]), "+v"(S[7])::"memory")
DI void wo_piece(int i, unsigned m0v, unsigned v, rsrc_t r, unsigned so) {
  if (i == 0) dma1<0, 0>(m0v, v, r, so);
  if (i == 1) dma1<1, 0>(m0v, v + 7168u, r, so);
  if (i == 2) dma1<2, 0>(m0v, v + 14336u, r, so);
  if (i == 3) dma1<3, 0>(m0v, v + 21504u, r, so);
}
template <bool ISSUE>
DI void ostep(f32x16& acc0, f32x16& acc1, const bf16x8 (&SC)[8], bf16x8 (&SN)[8], unsigned a0, unsigned m0v, unsigned voffWo,
              rsrc_t rwo, unsigned so, unsigned vo, unsigned vo2, const char* onext) {
  bf16x8 f[16];
  const unsigned a1 = a0 ^ 64u;
#define OFRAG(i) lds_tr2((((i) & 1) ? a1 : a0) + 2048u * ((i) >> 1), (((i) & 1) ? a1 : a0) + 2048u * ((i) >> 1) + 1024u)
  f[0] = OFRAG(0); f[1] = OFRAG(1); f[2] = OFRAG(2); f[3] = OFRAG(3);
  SB();
#pragma unroll
  for (unsigned i = 0; i < 16; i += 2) {
    if (i + 4 < 16) { f[i + 4] = OFRAG(i + 4); f[i + 5] = OFRAG(i + 5); }
    SB();
    acc0 = MFMA(f[i], SC[i >> 1], acc0);
    SB();
    if (ISSUE) { if (i < 8) wo_piece(i >> 1, m0v, voffWo, rwo, so); }
    SB();
    acc1 = MFMA(f[i + 1], SC[i >> 1], acc1);
    SB();
    if (ISSUE) ldo_rt(SN, i >> 1, vo, vo2, onext);
    SB();
  }
#undef OFRAG
}

__global__ __launch_bounds__(512, 2) void oproj_kernel(const __bf16* __restrict__ ows, const __bf16* __restrict__ woA,
                                                       const __bf16* __restrict__ woB, __bf16* __restrict__ out) {
  extern __shared__ __attribute__((aligned(16))) char smem[];
  const unsigned ldsb = (unsigned)(size_t)(LAS char*)smem;
  const unsigned tid = threadIdx.x, lane = tid & 63u;
  const unsigned w8 = __builtin_amdgcn_readfirstlane(tid >> 6);
  const unsigned w = w8 & 3u, g = w8 >> 2;
  const unsigned bid = blockIdx.x;
  const unsigned xcd = bid & 7u, idx = bid >> 3;
  const unsigned pair = xcd * 4u + (idx >> 3), nt = idx & 7u;
  const unsigned br = pair >> 4, b = pair & 15u;
  const __bf16* wo = (br ? woB : woA) + nt * 64u;
  const rsrc_t rwo = __builtin_amdgcn_make_buffer_rsrc((void*)wo, 0, ((NH * DH - 1) * DM + 64) * 2, 0x00020000);
  const unsigned voffWo = (32u * w + (lane >> 3)) * 1024u + (((lane & 7u) ^ (((lane >> 4) & 1u) << 2)) << 4);
  const unsigned hh = lane >> 5, q4 = (lane >> 2) & 3u, p4 = lane & 3u, dhalf = (lane >> 4) & 1u;
  const unsigned coloff = 16u * (p4 & 1u) + 8u * dhalf + 4u * (p4 >> 1);
  const unsigned ring = ldsb + g * (4u * SLOT);
  const unsigned lbA = ring + (4u * hh + q4) * 128u + (((q4 >> 1) & 1u) << 6) + 2u * coloff;
  const unsigned m0w = ring + w * 4096u;
  const char* obase = (const char*)ows + ((size_t)(pair * NH + 16u * g) * 4u + w) * 8192u;
  const unsigned wso = 16u * g * 131072u;
  const unsigned vo = lane * 16u, vo2 = lane * 16u + 4096u;

  bf16x8 B0[8], B1[8], B2[8], B3[8];
  f32x16 acc0 = {}, acc1 = {};
#pragma unroll
  for (int i = 0; i < 4; ++i) wo_piece(i, m0w, voffWo, rwo, wso);
#pragma unroll
  for (int i = 0; i < 8; ++i) ldo_rt(B0, i, vo, vo2, obase);
#pragma unroll
  for (int i = 0; i < 4; ++i) wo_piece(i, m0w + SLOT, voffWo, rwo, wso + 131072u);
#pragma unroll
  for (int i = 0; i < 8; ++i) ldo_rt(B1, i, vo, vo2, obase + 32768);
#pragma unroll
  for (int i = 0; i < 4; ++i) wo_piece(i, m0w + 2 * SLOT, voffWo, rwo, wso + 2u * 131072u);
#pragma unroll
  for (int i = 0; i < 8; ++i) ldo_rt(B2, i, vo, vo2, obase + 2 * 32768);

#define STEP(ISS, hd, SC, SN, WN)                                                          \
  do {                                                                                     \
    WAITO(WN, SC);                                                                         \
    ostep<ISS>(acc0, acc1, SC, SN, lbA + ((hd) & 3u) * SLOT, m0w + (((hd) + 3u) & 3u) * SLOT, voffWo, rwo, \
               wso + ((hd) + 3u) * 131072u, vo, vo2, obase + (size_t)((hd) + 3u) * 32768u); \
  } while (0)
  for (unsigned hd = 0; hd < 12; hd += 4) {
    STEP(true, hd, B0, B3, 24);
    STEP(true, hd + 1, B1, B0, 24);
    STEP(true, hd + 2, B2, B1, 24);
    STEP(true, hd + 3, B3, B2, 24);
  }
  STEP(true, 12u, B0, B3, 24);
  STEP(false, 13u, B1, B0, 24);
  STEP(false, 14u, B2, B1, 12);
  STEP(false, 15u, B3, B2, 0);
#undef STEP
  BARRIER();
  typedef __attribute__((ext_vector_type(4))) float f32x4;
  LAS f32x4* red = (LAS f32x4*)(ldsb + w * 8192u) + lane;
  if (g == 1) {
#pragma unroll
    for (int i = 0; i < 4; ++i) {
      red[i * 64] = f32x4{acc0[4 * i], acc0[4 * i + 1], acc0[4 * i + 2], acc0[4 * i + 3]};
      red[(4 + i) * 64] = f32x4{acc1[4 * i], acc1[4 * i + 1], acc1[4 * i + 2], acc1[4 * i + 3]};
    }
  }
  BARRIER();
  if (g == 0) {
#pragma unroll
    for (int i = 0; i < 4; ++i) {
      f32x4 a = red[i * 64], c2 = red[(4 + i) * 64];
#pragma unroll
      for (int e = 0; e < 4; ++e) { acc0[4 * i + e] += a[e]; acc1[4 * i + e] += c2[e]; }
    }
    const unsigned r32 = lane & 31u;
    __bf16* orow = out + ((size_t)b * SQ + 32u * w + r32) * 1024u + br * 512u + nt * 64u + 16u * hh;
    *(bf16x8*)(orow) = pack8n(acc0, 0);
    *(bf16x8*)(orow + 8) = pack8n(acc0, 8);
    *(bf16x8*)(orow + 32) = pack8n(acc1, 0);
    *(bf16x8*)(orow + 40) = pack8n(acc1, 8);
  }
}

extern "C" void kernel_launch(void* const* d_in, const int* in_sizes, int n_in, void* d_out, int out_size, void* d_ws,
                              size_t ws_size, hipStream_t stream) {
  const __bf16* xA = (const __bf16*)d_in[0];
  const __bf16* kA = (const __bf16*)d_in[1];
  const __bf16* vA = (const __bf16*)d_in[2];
  const __bf16* wqA = (const __bf16*)d_in[3];
  const __bf16* woA = (const __bf16*)d_in[4];
  const __bf16* xB = (const __bf16*)d_in[5];
  const __bf16* kB = (const __bf16*)d_in[6];
  const __bf16* vB = (const __bf16*)d_in[7];
  const __bf16* wqB = (const __bf16*)d_in[8];
  const __bf16* woB = (const __bf16*)d_in[9];
  __bf16* ows = (__bf16*)d_ws;
  hipFuncSetAttribute((const void*)attn_kernel, hipFuncAttributeMaxDynamicSharedMemorySize, NSLOT * SLOT);
  hipFuncSetAttribute((const void*)oproj_kernel, hipFuncAttributeMaxDynamicSharedMemorySize, 8 * SLOT);
  hipLaunchKernelGGL(attn_kernel, dim3(512), dim3(256), NSLOT * SLOT, stream, xA, kA, vA, wqA, woA, xB, kB, vB, wqB, woB, ows);
  hipLaunchKernelGGL(oproj_kernel, dim3(256), dim3(512), 8 * SLOT, stream, (const __bf16*)ows, woA, woB, (__bf16*)d_out);
}
```

```cpp
#include <hip/hip_runtime.h>
#include <hip/hip_bf16.h>
#include <stdint.h>

typedef __attribute__((ext_vector_type(8))) __bf16 bf16x8;
typedef __attribute__((ext_vector_type(4))) __bf16 bf16x4;
typedef __attribute__((ext_vector_type(2))) __bf16 bf16x2;
typedef __attribute__((ext_vector_type(16))) float f32x16;
typedef __attribute__((ext_vector_type(2))) float f32x2;
typedef __attribute__((ext_vector_type(4))) unsigned u32x4;
#define LAS __attribute__((address_space(3)))
#define DI __device__ __forceinline__
#define AI __attribute__((always_inline))
typedef __amdgpu_buffer_rsrc_t rsrc_t;

constexpr int NB = 16, SQ = 128, DM = 512, NH = 32, SK = 512, DH = 128;
constexpr unsigned SLOT = 16384, NSLOT = 5;

#define WAITV(n) asm volatile("s_waitcnt vmcnt(" #n ")" ::: "memory")
#define BARRIER() asm volatile("s_waitcnt lgkmcnt(0)\n\ts_barrier" ::: "memory")

DI void dma16(unsigned m0v, unsigned voff, rsrc_t r, unsigned soff) {
  asm volatile("s_mov_b32 m0, %0\n\ts_nop 0\n\tbuffer_load_dwordx4 %1, %2, %3 offen lds"
               :: "s"(m0v), "v"(voff), "s"(r), "s"(soff) : "memory");
}
DI void dma16nt(unsigned m0v, unsigned voff, rsrc_t r, unsigned soff) {
  asm volatile("s_mov_b32 m0, %0\n\ts_nop 0\n\tbuffer_load_dwordx4 %1, %2, %3 offen nt lds"
               :: "s"(m0v), "v"(voff), "s"(r), "s"(soff) : "memory");
}
DI unsigned cvtpk(float lo, float hi) {
  f32x2 v = {lo, hi};
  bf16x2 b = __builtin_convertvector(v, bf16x2);
  return __builtin_bit_cast(unsigned, b);
}
DI bf16x8 pack8(const f32x16& a, int base, float sc) {
  u32x4 w = {cvtpk(a[base + 0] * sc, a[base + 1] * sc), cvtpk(a[base + 2] * sc, a[base + 3] * sc),
             cvtpk(a[base + 4] * sc, a[base + 5] * sc), cvtpk(a[base + 6] * sc, a[base + 7] * sc)};
  return __builtin_bit_cast(bf16x8, w);
}
DI bf16x8 pack8n(const f32x16& a, int base) {
  u32x4 w = {cvtpk(a[base + 0], a[base + 1]), cvtpk(a[base + 2], a[base + 3]),
             cvtpk(a[base + 4], a[base + 5]), cvtpk(a[base + 6], a[base + 7])};
  return __builtin_bit_cast(bf16x8, w);
}
DI bf16x8 lds_b128(unsigned addr) { return *(LAS const bf16x8*)(addr); }
DI bf16x8 lds_tr2(unsigned addr0, unsigned addr1) {
  bf16x4 lo = __builtin_amdgcn_ds_read_tr16_b64_v4bf16((LAS bf16x4*)(addr0));
  bf16x4 hi = __builtin_amdgcn_ds_read_tr16_b64_v4bf16((LAS bf16x4*)(addr1));
  return __builtin_shufflevector(lo, hi, 0, 1, 2, 3, 4, 5, 6, 7);
}
DI float xhalf_max(float x) {
  auto r = __builtin_amdgcn_permlane32_swap(__float_as_uint(x), __float_as_uint(x), false, false);
  return fmaxf(__uint_as_float(r[0]), __uint_as_float(r[1]));
}
DI float xhalf_sum(float x) {
  auto r = __builtin_amdgcn_permlane32_swap(__float_as_uint(x), __float_as_uint(x), false, false);
  return __uint_as_float(r[0]) + __uint_as_float(r[1]);
}
#define MFMA(a, b, c) __builtin_amdgcn_mfma_f32_32x32x16_bf16((a), (b), (c), 0, 0, 0)

#define SB() __builtin_amdgcn_sched_barrier(0)
DI unsigned cvtpk_pin(float lo, float hi) { unsigned t = cvtpk(lo, hi); asm volatile("" : "+v"(t)); return t; }
#define PIN(x) asm volatile("" : "+v"(x))
#define PIN2(x, y) asm volatile("" : "+v"(x), "+v"(y))
#define ADD0(acc, x) acc += (x)
#define ADD1(acc, x) acc = __builtin_fmaf((x), one, acc)
template <int I, int NT> DI void dma1(unsigned m0v, unsigned v, rsrc_t r, unsigned soff) {
  if (NT) asm volatile("s_mov_b32 m0, %0\n\ts_nop 0\n\tbuffer_load_dwordx4 %1, %2, %3 offen offset:%4 nt lds"
                       :: "s"(m0v), "v"(v), "s"(r), "s"(soff), "i"(I * 1024) : "memory");
  else asm volatile("s_mov_b32 m0, %0\n\ts_nop 0\n\tbuffer_load_dwordx4 %1, %2, %3 offen offset:%4 lds"
                    :: "s"(m0v), "v"(v), "s"(r), "s"(soff), "i"(I * 1024) : "memory");
}
DI void xld(bf16x8& d, unsigned v, rsrc_t r, unsigned soff) {
  asm volatile("buffer_load_dwordx4 %0, %1, %2, %3 offen" : "=v"(d) : "v"(v), "s"(r), "s"(soff) : "memory");
}
enum { TNONE = 0, TW = 1, TK = 2, TV = 3 };

struct Ctx {
  rsrc_t rx, rw, rk, rv;
  unsigned voffKV, voffW, voffX, lbK, lbV, lbW, lbX, lwX, m0w;
  float one;
};
template <int T, int I> DI void piece(const Ctx& c, unsigned slot, unsigned so) {
  if (T == TW) dma1<I, 0>(c.m0w + slot, (c.voffW ^ ((I >> 1) << 5)) + (I >> 1) * 65536u + (I & 1) * 128u + 4096u - I * 1024u, c.rw, so);
  if (T == TK) dma1<I, 1>(c.m0w + slot, (c.voffKV ^ ((I >> 1) << 5)) + (I >> 1) * 2048u + (I & 1) * 128u + 4096u - I * 1024u, c.rk, so);
  if (T == TV) dma1<I, 1>(c.m0w + slot, (c.voffKV ^ ((I >> 1) << 5)) + (I >> 1) * 2048u + (I & 1) * 128u + 4096u - I * 1024u, c.rv, so);
}
template <int T> DI void piece_rt(const Ctx& c, int i, unsigned slot, unsigned so) {
  if (i == 0) piece<T, 0>(c, slot, so);
  if (i == 1) piece<T, 1>(c, slot, so);
  if (i == 2) piece<T, 2>(c, slot, so);
  if (i == 3) piece<T, 3>(c, slot, so);
}
template <int T> DI void tile_all(const Ctx& c, unsigned slot, unsigned so) {
  piece<T, 0>(c, slot, so); piece<T, 1>(c, slot, so); piece<T, 2>(c, slot, so); piece<T, 3>(c, slot, so);
}
DI void xld1(const Ctx& c, bf16x8 (&x)[4], int i, unsigned so) { xld(x[i], c.voffX, c.rx, so + i * 8192u); }
DI void xld_all(const Ctx& c, bf16x8 (&x)[4], unsigned so) { xld1(c, x, 0, so); xld1(c, x, 1, so); xld1(c, x, 2, so); xld1(c, x, 3, so); }
DI void xwr(unsigned lw, const bf16x8 (&x)[4]) {
#pragma unroll
  for (unsigned i = 0; i < 4; ++i) *(LAS bf16x8*)((lw ^ ((i & 1u) << 6)) + i * 1024u) = x[i];
}
DI bf16x8 xfrag(unsigned lb, unsigned ks) { return lds_b128(lb ^ (ks << 5)); }
#define TIE4(X) "+v"(X[0]), "+v"(X[1]), "+v"(X[2]), "+v"(X[3])
#define SYNCX(n, X) asm volatile("s_waitcnt vmcnt(" #n ") lgkmcnt(0)\n\ts_barrier" : TIE4(X) :: "memory")
#define SYNCX2(n, X, Y) asm volatile("s_waitcnt vmcnt(" #n ") lgkmcnt(0)\n\ts_barrier" : TIE4(X), TIE4(Y) :: "memory")
#define SYNC(n) asm volatile("s_waitcnt vmcnt(" #n ") lgkmcnt(0)\n\ts_barrier" ::: "memory")

constexpr float SMC = 0.08838834764831845f * 1.4426950408889634f;
constexpr float THR = 8.0f;
#define EXP2(S, R) S[R] = __builtin_amdgcn_exp2f(S[R])
DI bf16x8 kfrag(unsigned a0, unsigned a1, unsigned i) {
  const unsigned s = i >> 1;
  return lds_b128(((s & 1u) ? a1 : a0) + 8192u * (i & 1u) + 512u * (s >> 1));
}
DI bf16x8 vfrag(unsigned a0, unsigned a1, unsigned i) {
  const unsigned o = 4096u * (i >> 2) + 512u * (i & 3u);
  return lds_tr2(a0 + o, a1 + o + 2048u);
}
DI bf16x8 wfrag(unsigned a0, unsigned a1, unsigned i) {
  const unsigned o = 4096u * (i >> 2) + 512u * (i & 3u);
  return lds_tr2(a0 + o, a1 + o + 256u);
}

template <int T0, int T1, bool XW, bool XL>
DI void qphase(const Ctx& c, f32x16 (&acc)[4], const bf16x8 (&xb)[4], bf16x8 (&xn)[4], const bf16x8 (&xw)[4], bf16x8 (&xl)[4],
               unsigned sc, unsigned s0, unsigned so0, unsigned s1, unsigned so1, unsigned lbx, unsigned lwx, unsigned xso) {
  const unsigned a0 = c.lbW + sc, a1 = a0 ^ 16u;
  bf16x8 af[16];
  af[0] = wfrag(a0, a1, 0); af[1] = wfrag(a0, a1, 1); af[2] = wfrag(a0, a1, 2); af[3] = wfrag(a0, a1, 3);
  if (XW) xwr(lwx, xw);
  piece<T0, 0>(c, s0, so0);
  SB();
#pragma unroll
  for (unsigned i = 0; i < 16; i += 2) {
    if (i + 4 < 16) { af[i + 4] = wfrag(a0, a1, i + 4); af[i + 5] = wfrag(a0, a1, i + 5); }
    if (XW && i == 12) { xn[0] = xfrag(lbx, 0); xn[1] = xfrag(lbx, 1); }
    if (XW && i == 14) { xn[2] = xfrag(lbx, 2); xn[3] = xfrag(lbx, 3); }
    SB();
    acc[i & 3] = MFMA(af[i], xb[i >> 2], acc[i & 3]);
    SB();
    if (i < 6) piece_rt<T0>(c, (i >> 1) + 1, s0, so0);
    else if (i < 14) {
      if (XL) xld1(c, xl, (i - 6) >> 1, xso);
      if (T1 != TNONE) piece_rt<(T1 != TNONE ? T1 : TW)>(c, (i - 6) >> 1, s1, so1);
    }
    SB();
    acc[(i + 1) & 3] = MFMA(af[i + 1], xb[i >> 2], acc[(i + 1) & 3]);
    SB();
  }
}

template <bool FIN, int T0>
DI void aphase(const Ctx& c, const bf16x8 (&qf)[8], const f32x16& negm, f32x16& n0, f32x16& n1, f32x16& p0, f32x16& p1,
               u32x4 (&pw)[4], float& l, unsigned sc, unsigned s0, unsigned so0) {
  const unsigned a0 = c.lbK + sc, a1 = a0 ^ 32u;
  const float one = c.one;
  bf16x8 kf[16];
  kf[0] = kfrag(a0, a1, 0); kf[1] = kfrag(a0, a1, 1); kf[2] = kfrag(a0, a1, 2); kf[3] = kfrag(a0, a1, 3);
  float ps0 = l, ps1 = 0.f;
  piece<T0, 0>(c, s0, so0);
  if (FIN) {
#pragma unroll
    for (int e = 0; e < 8; ++e) pw[e >> 2][e & 3] = cvtpk_pin(p0[2 * e], p0[2 * e + 1]);
  }
  SB();
#pragma unroll
  for (unsigned i = 0; i < 16; i += 2) {
    if (i + 4 < 16) { kf[i + 4] = kfrag(a0, a1, i + 4); kf[i + 5] = kfrag(a0, a1, i + 5); }
    SB();
    if (i == 0) { if (FIN) n0 = MFMA(kf[i], qf[i >> 1], negm); else { f32x16 z = {}; n0 = MFMA(kf[i], qf[i >> 1], z); } }
    else n0 = MFMA(kf[i], qf[i >> 1], n0);
    SB();
    if (FIN) { EXP2(p1, i); EXP2(p1, i + 1); PIN2(p1[i], p1[i + 1]); }
    if (i < 6) piece_rt<T0>(c, (i >> 1) + 1, s0, so0);
    SB();
    if (i == 0) { if (FIN) n1 = MFMA(kf[i + 1], qf[i >> 1], negm); else { f32x16 z = {}; n1 = MFMA(kf[i + 1], qf[i >> 1], z); } }
    else n1 = MFMA(kf[i + 1], qf[i >> 1], n1);
    SB();
    if (FIN) {
      pw[2 + (i >> 3)][(i >> 1) & 3] = cvtpk_pin(p1[i], p1[i + 1]);
      ADD0(ps0, p1[i]);
      if (i == 0) ps1 = p1[1]; else ADD1(ps1, p1[i + 1]);
      PIN2(ps0, ps1);
    }
    SB();
  }
  if (FIN) l = ps0 + ps1;
}

DI float row_max(f32x16& s0, f32x16& s1) {
  float a, b;
  asm("v_max3_f32 %0, %2, %3, %4\n\tv_max3_f32 %1, %15, %16, %17\n\t"
      "v_max3_f32 %0, %0, %5, %6\n\tv_max3_f32 %1, %1, %18, %19\n\t"
      "v_max3_f32 %0, %0, %7, %8\n\tv_max3_f32 %1, %1, %20, %21\n\t"
      "v_max3_f32 %0, %0, %9, %10\n\tv_max3_f32 %1, %1, %22, %23\n\t"
      "v_max3_f32 %0, %0, %11, %12\n\tv_max3_f32 %1, %1, %24, %25\n\t"
      "v_max3_f32 %0, %0, %13, %14\n\tv_max3_f32 %1, %1, %26, %27"
      : "=&v"(a), "=&v"(b)
      : "v"(s0[0]), "v"(s0[1]), "v"(s0[2]), "v"(s0[3]), "v"(s0[4]), "v"(s0[5]), "v"(s0[6]), "v"(s0[7]), "v"(s0[8]), "v"(s0[9]),
        "v"(s0[10]), "v"(s0[11]), "v"(s0[12]),
        "v"(s1[0]), "v"(s1[1]), "v"(s1[2]), "v"(s1[3]), "v"(s1[4]), "v"(s1[5]), "v"(s1[6]), "v"(s1[7]), "v"(s1[8]), "v"(s1[9]),
        "v"(s1[10]), "v"(s1[11]), "v"(s1[12]));
  asm("v_max3_f32 %0, %0, %2, %3\n\tv_max3_f32 %1, %1, %4, %5\n\t"
      "v_max3_f32 %0, %0, %6, %7\n\tv_max_f32_e32 %0, %0, %1\n\t"
      "v_mov_b32_e32 %1, %0\n\ts_nop 1\n\tv_permlane32_swap_b32_e32 %0, %1\n\tv_max_f32_e32 %0, %0, %1"
      : "+v"(a), "+v"(b)
      : "v"(s0[13]), "v"(s0[14]), "v"(s1[13]), "v"(s1[14]), "v"(s0[15]), "v"(s1[15]));
  return a;
}

template <bool LAST, int T0, int XL>
DI void bphase(const Ctx& c, f32x16 (&o)[4], f32x16& negm, f32x16& s0, f32x16& s1, u32x4 (&pw)[4], float& l,
               unsigned sc, unsigned sl0, unsigned so0, bf16x8 (&xa)[4], bf16x8 (&xb)[4], unsigned xso) {
  const unsigned a0 = c.lbV + sc, a1 = a0 ^ 32u;
  const float one = c.one;
  bf16x8 vf[16];
  vf[0] = vfrag(a0, a1, 0); vf[1] = vfrag(a0, a1, 1); vf[2] = vfrag(a0, a1, 2); vf[3] = vfrag(a0, a1, 3);
  float ps0 = 0.f, ps1 = 0.f, alpha = 1.f;
  bool resc = false;
  if (T0 != TNONE) piece<(T0 != TNONE ? T0 : TW), 0>(c, sl0, so0);
  if (LAST) {
#pragma unroll
    for (int e = 0; e < 8; ++e) pw[e >> 2][e & 3] = cvtpk_pin(s0[2 * e], s0[2 * e + 1]);
  } else {
    asm volatile("s_nop 3" : "+v"(s0), "+v"(s1));
    const float rmax = row_max(s0, s1);
    if (__builtin_expect(__any(rmax > THR), 0)) {
      const float dl = fmaxf(rmax, 0.f);
      alpha = __builtin_amdgcn_exp2f(-dl);
      l *= alpha; resc = true;
#pragma unroll
      for (int r = 0; r < 16; ++r) { s0[r] -= dl; s1[r] -= dl; }
      asm volatile("v_sub_f32 %0, %0, %16\n\tv_sub_f32 %1, %1, %16\n\tv_sub_f32 %2, %2, %16\n\tv_sub_f32 %3, %3, %16\n\t"
                   "v_sub_f32 %4, %4, %16\n\tv_sub_f32 %5, %5, %16\n\tv_sub_f32 %6, %6, %16\n\tv_sub_f32 %7, %7, %16\n\t"
                   "v_sub_f32 %8, %8, %16\n\tv_sub_f32 %9, %9, %16\n\tv_sub_f32 %10, %10, %16\n\tv_sub_f32 %11, %11, %16\n\t"
                   "v_sub_f32 %12, %12, %16\n\tv_sub_f32 %13, %13, %16\n\tv_sub_f32 %14, %14, %16\n\tv_sub_f32 %15, %15, %16\n\ts_nop 1"
                   : "+v"(negm[0]), "+v"(negm[1]), "+v"(negm[2]), "+v"(negm[3]), "+v"(negm[4]), "+v"(negm[5]), "+v"(negm[6]), "+v"(negm[7]),
                     "+v"(negm[8]), "+v"(negm[9]), "+v"(negm[10]), "+v"(negm[11]), "+v"(negm[12]), "+v"(negm[13]), "+v"(negm[14]), "+v"(negm[15])
                   : "v"(dl));
    }
  }
  SB();
#pragma unroll
  for (unsigned i = 0; i < 16; i += 2) {
    const unsigned ks = i >> 2, db = i & 3u;
    if (i + 4 < 16) { vf[i + 4] = vfrag(a0, a1, i + 4); vf[i + 5] = vfrag(a0, a1, i + 5); }
    SB();
    o[db] = MFMA(vf[i], __builtin_bit_cast(bf16x8, pw[ks]), o[db]);
    SB();
    if (i < 6) { if (T0 != TNONE) piece_rt<(T0 != TNONE ? T0 : TW)>(c, (i >> 1) + 1, sl0, so0); }
    else if (i < 14 && XL >= 1) xld1(c, xa, (i - 6) >> 1, xso);
    if (LAST) {
      if (i < 8) {
        EXP2(s1, 2 * i); EXP2(s1, 2 * i + 1);
        pw[2 + (i >> 2)][i & 3] = cvtpk_pin(s1[2 * i], s1[2 * i + 1]);
        if (i == 0) { ps0 = s1[0]; ps1 = s1[1]; } else { ADD0(ps0, s1[2 * i]); ADD1(ps1, s1[2 * i + 1]); }
        PIN2(ps0, ps1);
      }
    } else {
      EXP2(s0, i); EXP2(s0, i + 1); PIN2(s0[i], s0[i + 1]);
    }
    SB();
    o[db + 1] = MFMA(vf[i + 1], __builtin_bit_cast(bf16x8, pw[ks]), o[db + 1]);
    SB();
    if (i >= 6 && i < 14 && XL >= 2) xld1(c, xb, (i - 6) >> 1, xso + 128u);
    if (LAST) {
      if (i < 8) {
        EXP2(s1, 2 * i + 2); EXP2(s1, 2 * i + 3);
        pw[2 + (i >> 2)][(i & 3) + 1] = cvtpk_pin(s1[2 * i + 2], s1[2 * i + 3]);
        ADD0(ps0, s1[2 * i + 2]); ADD1(ps1, s1[2 * i + 3]);
        PIN2(ps0, ps1);
      }
    } else {
      if (i == 0) { ps0 = s0[0]; ps1 = s0[1]; } else { ADD0(ps0, s0[i]); ADD1(ps1, s0[i + 1]); }
      PIN2(ps0, ps1);
    }
    SB();
  }
  l += ps0 + ps1;
  if (!LAST) {
    if (__builtin_expect(resc, 0)) {
#pragma unroll
      for (int db = 0; db < 4; ++db)
#pragma unroll
        for (int r = 0; r < 16; ++r) o[db][r] *= alpha;
    }
  }
}

__global__ __launch_bounds__(256, 2) void attn_kernel(
    const __bf16* __restrict__ xA, const __bf16* __restrict__ kA, const __bf16* __restrict__ vA, const __bf16* __restrict__ wqA,
    const __bf16* __restrict__ woA,
    const __bf16* __restrict__ xB, const __bf16* __restrict__ kB, const __bf16* __restrict__ vB, const __bf16* __restrict__ wqB,
    const __bf16* __restrict__ woB, __bf16* __restrict__ ows) {
  extern __shared__ __attribute__((aligned(16))) char smem[];
  const unsigned ldsb = (unsigned)(size_t)(LAS char*)smem;
  const unsigned tid = threadIdx.x, lane = tid & 63u;
  const unsigned w = __builtin_amdgcn_readfirstlane(tid >> 6);
  const unsigned bid = blockIdx.x;
  const unsigned typeB = (__builtin_amdgcn_s_getreg(6 | (11 << 11)) != 0) ? 1u : 0u;
  const unsigned xcd = bid & 7u, idx = bid >> 3;
  const unsigned br = xcd >> 2, h = (xcd & 3u) * 8u + (idx & 7u), b0 = idx >> 3;
  Ctx c;
  c.rx = __builtin_amdgcn_make_buffer_rsrc((void*)(br ? xB : xA), 0, NB * SQ * DM * 2, 0x00020000);
  c.rw = __builtin_amdgcn_make_buffer_rsrc((void*)((const char*)((br ? wqB : wqA) + (size_t)h * DH) - 4096), 0, ((DM - 1) * NH * DH + DH) * 2 + 4096, 0x00020000);
  c.rk = __builtin_amdgcn_make_buffer_rsrc((void*)((const char*)(br ? kB : kA) - 4096), 0, NB * NH * SK * DH * 2 + 4096, 0x00020000);
  c.rv = __builtin_amdgcn_make_buffer_rsrc((void*)((const char*)(br ? vB : vA) - 4096), 0, NB * NH * SK * DH * 2 + 4096, 0x00020000);
  const unsigned l4 = lane >> 4;
  const unsigned r32 = lane & 31u, hh = lane >> 5;
  const unsigned q4 = (lane >> 2) & 3u, p4 = lane & 3u, dhalf = (lane >> 4) & 1u;
  {
    const unsigned row = 16u * w + ((lane >> 2) & 7u), col = hh * 64u + ((p4 ^ dhalf) << 4);
    c.voffKV = row * 256u + col;
    c.voffW = row * 8192u + col;
  }
  c.voffX = (32u * w + (lane >> 3)) * 1024u + ((lane & 7u) << 4);
  c.lbK = ldsb + 2048u * (r32 >> 3) + 64u * (r32 & 7u) + ((hh ^ ((r32 >> 2) & 3u)) << 4);
  c.lbV = ldsb + 64u * (4u * hh + q4) + (((2u * dhalf + (p4 >> 1)) ^ hh) << 4) + 8u * (p4 & 1u);
  c.lbW = ldsb + 2048u * hh + 64u * q4 + (((2u * dhalf + (p4 & 1u)) ^ (2u * hh)) << 4) + 8u * (p4 >> 1);
  c.lbX = ldsb + w * 4096u + r32 * 128u + ((hh ^ ((r32 >> 1) & 7u)) << 4);
  c.lwX = ldsb + w * 4096u + (lane >> 3) * 128u + (((lane & 7u) ^ l4) << 4);
  c.m0w = ldsb + w * 4096u;
  c.one = 1.0f;
  asm volatile("" : "+v"(c.one));
  const float one = c.one;
  constexpr unsigned WSTEP = 64u * 8192u, OOB = 0x7f000000u, KT = 16384u;

  bf16x8 r0[4], r1[4], r2[4];
  unsigned f0 = 0, f1 = SLOT, f2 = 2 * SLOT, f3 = 3 * SLOT, xs = 4 * SLOT;
  tile_all<TW>(c, f0, 0);
  tile_all<TW>(c, f1, WSTEP);
  tile_all<TW>(c, f2, 2u * WSTEP); xld_all(c, r0, b0 * (SQ * DM * 2)); xld_all(c, r1, b0 * (SQ * DM * 2) + 128u);
  xld_all(c, r2, b0 * (SQ * DM * 2) + 256u);
  if (!typeB) __builtin_amdgcn_s_setprio(2);

  unsigned dummy = 0;
  for (unsigned unit = 0; unit < 2; ++unit) {
    const unsigned b = b0 + 8u * unit;
    const unsigned xso = b * (SQ * DM * 2);
    const unsigned kv = (b * NH + h) * (SK * DH * 2);
    const unsigned xnx = unit == 0 ? xso + 8u * (SQ * DM * 2) : OOB;
    const unsigned wnx = unit == 0 ? 0u : OOB;
    bf16x8 qf[8];
    {
      f32x16 acc[4] = {};
      bf16x8 fa[4], fb[4];
      const unsigned lbx = c.lbX + xs, lwx = c.lwX + xs;
      if (unit == 0) WAITV(4);
      SYNCX2(13, r0, r1);
      xwr(lwx, r0);
      fa[0] = xfrag(lbx, 0); fa[1] = xfrag(lbx, 1); fa[2] = xfrag(lbx, 2); fa[3] = xfrag(lbx, 3);
      qphase<TW, TNONE, true, true>(c, acc, fa, fb, r1, r0, f0, f3, 3u * WSTEP, 0, 0, lbx, lwx, xso + 3u * 128u);
      if (unit == 0) WAITV(8);
      SYNCX(17, r2);
      qphase<TW, TNONE, true, true>(c, acc, fb, fa, r2, r1, f1, f0, 4u * WSTEP, 0, 0, lbx, lwx, xso + 4u * 128u);
      SYNCX(8, r0);
      PIN(dummy);
      qphase<TW, TNONE, true, true>(c, acc, fa, fb, r0, r2, f2, f1, 5u * WSTEP, 0, 0, lbx, lwx, xso + 5u * 128u);
      SYNCX(8, r1);
      qphase<TW, TNONE, true, true>(c, acc, fb, fa, r1, r0, f3, f2, 6u * WSTEP, 0, 0, lbx, lwx, xso + 6u * 128u);
      SYNCX(8, r2);
      qphase<TW, TNONE, true, true>(c, acc, fa, fb, r2, r1, f0, f3, 7u * WSTEP, 0, 0, lbx, lwx, xso + 7u * 128u);
      SYNCX(8, r0);
      qphase<TK, TNONE, true, false>(c, acc, fb, fa, r0, r2, f1, f0, kv, 0, 0, lbx, lwx, 0);
      SYNCX(4, r1);
      qphase<TK, TNONE, true, false>(c, acc, fa, fb, r1, r2, f2, f1, kv + KT, 0, 0, lbx, lwx, 0);
      SYNC(12);
      qphase<TV, TK, false, false>(c, acc, fb, fa, r1, r2, f3, f2, kv, xs, kv + 2u * KT, lbx, lwx, 0);
#pragma unroll
      for (int db = 0; db < 4; ++db) { qf[2 * db] = pack8(acc[db], 0, SMC); qf[2 * db + 1] = pack8(acc[db], 8, SMC); }
    }
    __builtin_amdgcn_s_setprio(0);
    unsigned g0 = f0, g1 = f1, g2 = f2, g3 = xs, g4 = f3;
    f32x16 o[4] = {};
    f32x16 sA0, sA1, sB0, sB1, negm;
    u32x4 pw[4];
    float l = 0.f;
    SYNC(12);
    aphase<false, TV>(c, qf, negm, sA0, sA1, sB0, sB1, pw, l, g0, g4, kv + KT);
    {
      asm volatile("s_nop 7\n\ts_nop 7" : "+v"(sA0), "+v"(sA1));
      const float m = row_max(sA0, sA1);
      float ps0 = 0.f, ps1 = 0.f;
#pragma unroll
      for (int r = 0; r < 16; ++r) { sA0[r] -= m; sA1[r] -= m; negm[r] = -m; }
      asm volatile("" : "+v"(negm));
#pragma unroll
      for (int r = 0; r < 16; r += 2) { EXP2(sA0, r); EXP2(sA0, r + 1); ADD0(ps0, sA0[r]); ADD1(ps1, sA0[r + 1]); }
      l = ps0 + ps1;
    }
    SYNC(12);
    aphase<true, TK>(c, qf, negm, sB0, sB1, sA0, sA1, pw, l, g1, g0, kv + 3u * KT);
    SYNC(12);
    bphase<false, TV, 0>(c, o, negm, sB0, sB1, pw, l, g2, g1, kv + 2u * KT, r0, r1, 0);
    { const unsigned t0 = g0, t1 = g1, t2 = g2; g0 = g3; g1 = g4; g2 = t0; g3 = t1; g4 = t2; }
    for (unsigned t = 2; t < 6; t += 2) {
      SYNC(12);
      aphase<true, TK>(c, qf, negm, sA0, sA1, sB0, sB1, pw, l, g0, g4, kv + (t + 2u) * KT);
      SYNC(12);
      bphase<false, TV, 0>(c, o, negm, sA0, sA1, pw, l, g1, g0, kv + (t + 1u) * KT, r0, r1, 0);
      SYNC(12);
      aphase<true, TK>(c, qf, negm, sB0, sB1, sA0, sA1, pw, l, g2, g1, kv + (t + 3u) * KT);
      SYNC(12);
      bphase<false, TV, 0>(c, o, negm, sB0, sB1, pw, l, g3, g2, kv + (t + 2u) * KT, r0, r1, 0);
      { const unsigned t4 = g4; g4 = g3; g3 = g2; g2 = g1; g1 = g0; g0 = t4; }
    }
    SYNC(12);
    aphase<true, TV>(c, qf, negm, sA0, sA1, sB0, sB1, pw, l, g0, g4, kv + 7u * KT);
    SYNC(12);
    bphase<false, TW, 0>(c, o, negm, sA0, sA1, pw, l, g1, g0, wnx, r0, r1, 0);
    SYNC(12);
    aphase<true, TW>(c, qf, negm, sB0, sB1, sA0, sA1, pw, l, g2, g1, wnx + WSTEP);
    SYNC(12);
    bphase<false, TW, 2>(c, o, negm, sB0, sB1, pw, l, g3, g2, wnx + 2u * WSTEP, r0, r1, xnx);
    SYNC(20);
    bphase<true, TNONE, 1>(c, o, negm, sB0, sB1, pw, l, g4, 0, 0, r2, r2, xnx + 256u);
    f0 = g0; f1 = g1; f2 = g2; f3 = g4; xs = g3;
    {
      const rsrc_t rwo = __builtin_amdgcn_make_buffer_rsrc((void*)(br ? woB : woA), 0, NH * DH * DM * 2, 0x00020000);
      asm volatile("buffer_load_dword %0, %1, %2, %3 offen" : "=v"(dummy)
                   : "v"(r32 * 128u), "s"(rwo), "s"(wnx + (((xcd & 3u) * 64u + idx) * 4u + w) * 4096u) : "memory");
    }
    const float inv = 1.0f / xhalf_sum(l);
    const unsigned u = (br * NB + b) * NH + h;
    bf16x8* op = (bf16x8*)ows + ((size_t)(u * 4u + w) * 8u) * 64u + lane;
#pragma unroll
    for (int db = 0; db < 4; ++db) {
      op[(2 * db) * 64] = pack8(o[db], 0, inv);
      op[(2 * db + 1) * 64] = pack8(o[db], 8, inv);
    }
    asm volatile("" ::: "memory");
  }
  asm volatile("s_waitcnt vmcnt(0)" : "+v"(dummy), TIE4(r0), TIE4(r1), TIE4(r2) :: "memory");
}

template <int IMM> DI void ldo(bf16x8& d, unsigned voff, const void* base) {
  asm volatile("global_load_dwordx4 %0, %1, %2 offset:%3" : "=v"(d) : "v"(voff), "s"(base), "i"(IMM) : "memory");
}
DI void ldo_rt(bf16x8 (&S)[8], int i, unsigned vo, unsigned vo2, const void* base) {
  if (i == 0) ldo<0>(S[0], vo, base);
  if (i == 1) ldo<1024>(S[1], vo, base);
  if (i == 2) ldo<2048>(S[2], vo, base);
  if (i == 3) ldo<3072>(S[3], vo, base);
  if (i == 4) ldo<0>(S[4], vo2, base);
  if (i == 5) ldo<1024>(S[5], vo2, base);
  if (i == 6) ldo<2048>(S[6], vo2, base);
  if (i == 7) ldo<3072>(S[7], vo2, base);
}
#define WAITO(n, S)                                                                       \
  asm volatile("s_waitcnt vmcnt(" #n ") lgkmcnt(0)\n\ts_barrier"                          \
               : "+v"(S[0]), "+v"(S[1]), "+v"(S[2]), "+v"(S[3]), "+v"(S[4]), "+v"(S[5]),  \
                 "+v"(S[6]), "+v"(S[7])::"memory")
DI void wo_piece(int i, unsigned m0v, unsigned v, rsrc_t r, unsigned so) {
  if (i == 0) dma1<0, 0>(m0v, v, r, so);
  if (i == 1) dma1<1, 0>(m0v, v + 7168u, r, so);
  if (i == 2) dma1<2, 0>(m0v, v + 14336u, r, so);
  if (i == 3) dma1<3, 0>(m0v, v + 21504u, r, so);
}
template <bool ISSUE>
DI void ostep(f32x16& acc0, f32x16& acc1, const bf16x8 (&SC)[8], bf16x8 (&SN)[8], unsigned a0, unsigned m0v, unsigned voffWo,
              rsrc_t rwo, unsigned so, unsigned vo, unsigned vo2, const char* onext) {
  bf16x8 f[16];
  const unsigned a1 = a0 ^ 64u;
#define OFRAG(i) lds_tr2((((i) & 1) ? a1 : a0) + 2048u * ((i) >> 1), (((i) & 1) ? a1 : a0) + 2048u * ((i) >> 1) + 1024u)
  f[0] = OFRAG(0); f[1] = OFRAG(1); f[2] = OFRAG(2); f[3] = OFRAG(3);
  SB();
#pragma unroll
  for (unsigned i = 0; i < 16; i += 2) {
    if (i + 4 < 16) { f[i + 4] = OFRAG(i + 4); f[i + 5] = OFRAG(i + 5); }
    SB();
    acc0 = MFMA(f[i], SC[i >> 1], acc0);
    SB();
    if (ISSUE) { if (i < 8) wo_piece(i >> 1, m0v, voffWo, rwo, so); }
    SB();
    acc1 = MFMA(f[i + 1], SC[i >> 1], acc1);
    SB();
    if (ISSUE) ldo_rt(SN, i >> 1, vo, vo2, onext);
    SB();
  }
#undef OFRAG
}

__global__ __launch_bounds__(512, 2) void oproj_kernel(const __bf16* __restrict__ ows, const __bf16* __restrict__ woA,
                                                       const __bf16* __restrict__ woB, __bf16* __restrict__ out) {
  extern __shared__ __attribute__((aligned(16))) char smem[];
  const unsigned ldsb = (unsigned)(size_t)(LAS char*)smem;
  const unsigned tid = threadIdx.x, lane = tid & 63u;
  const unsigned w8 = __builtin_amdgcn_readfirstlane(tid >> 6);
  const unsigned w = w8 & 3u, g = w8 >> 2;
  const unsigned bid = blockIdx.x;
  const unsigned xcd = bid & 7u, idx = bid >> 3;
  const unsigned pair = xcd * 4u + (idx >> 3), nt = idx & 7u;
  const unsigned br = pair >> 4, b = pair & 15u;
  const __bf16* wo = (br ? woB : woA) + nt * 64u;
  const rsrc_t rwo = __builtin_amdgcn_make_buffer_rsrc((void*)wo, 0, ((NH * DH - 1) * DM + 64) * 2, 0x00020000);
  const unsigned voffWo = (32u * w + (lane >> 3)) * 1024u + (((lane & 7u) ^ (((lane >> 4) & 1u) << 2)) << 4);
  const unsigned hh = lane >> 5, q4 = (lane >> 2) & 3u, p4 = lane & 3u, dhalf = (lane >> 4) & 1u;
  const unsigned coloff = 16u * (p4 & 1u) + 8u * dhalf + 4u * (p4 >> 1);
  const unsigned ring = ldsb + g * (4u * SLOT);
  const unsigned lbA = ring + (4u * hh + q4) * 128u + (((q4 >> 1) & 1u) << 6) + 2u * coloff;
  const unsigned m0w = ring + w * 4096u;
  const char* obase = (const char*)ows + ((size_t)(pair * NH + 16u * g) * 4u + w) * 8192u;
  const unsigned wso = 16u * g * 131072u;
  const unsigned vo = lane * 16u, vo2 = lane * 16u + 4096u;

  bf16x8 B0[8], B1[8], B2[8], B3[8];
  f32x16 acc0 = {}, acc1 = {};
#pragma unroll
  for (int i = 0; i < 4; ++i) wo_piece(i, m0w, voffWo, rwo, wso);
#pragma unroll
  for (int i = 0; i < 8; ++i) ldo_rt(B0, i, vo, vo2, obase);
#pragma unroll
  for (int i = 0; i < 4; ++i) wo_piece(i, m0w + SLOT, voffWo, rwo, wso + 131072u);
#pragma unroll
  for (int i = 0; i < 8; ++i) ldo_rt(B1, i, vo, vo2, obase + 32768);
#pragma unroll
  for (int i = 0; i < 4; ++i) wo_piece(i, m0w + 2 * SLOT, voffWo, rwo, wso + 2u * 131072u);
#pragma unroll
  for (int i = 0; i < 8; ++i) ldo_rt(B2, i, vo, vo2, obase + 2 * 32768);

#define STEP(ISS, hd, SC, SN, WN)                                                          \
  do {                                                                                     \
    WAITO(WN, SC);                                                                         \
    ostep<ISS>(acc0, acc1, SC, SN, lbA + ((hd) & 3u) * SLOT, m0w + (((hd) + 3u) & 3u) * SLOT, voffWo, rwo, \
               wso + ((hd) + 3u) * 131072u, vo, vo2, obase + (size_t)((hd) + 3u) * 32768u); \
  } while (0)
  for (unsigned hd = 0; hd < 12; hd += 4) {
    STEP(true, hd, B0, B3, 24);
    STEP(true, hd + 1, B1, B0, 24);
    STEP(true, hd + 2, B2, B1, 24);
    STEP(true, hd + 3, B3, B2, 24);
  }
  STEP(true, 12u, B0, B3, 24);
  STEP(false, 13u, B1, B0, 24);
  STEP(false, 14u, B2, B1, 12);
  STEP(false, 15u, B3, B2, 0);
#undef STEP
  BARRIER();
  typedef __attribute__((ext_vector_type(4))) float f32x4;
  LAS f32x4* red = (LAS f32x4*)(ldsb + w * 8192u) + lane;
  if (g == 1) {
#pragma unroll
    for (int i = 0; i < 4; ++i) {
      red[i * 64] = f32x4{acc0[4 * i], acc0[4 * i + 1], acc0[4 * i + 2], acc0[4 * i + 3]};
      red[(4 + i) * 64] = f32x4{acc1[4 * i], acc1[4 * i + 1], acc1[4 * i + 2], acc1[4 * i + 3]};
    }
  }
  BARRIER();
  if (g == 0) {
#pragma unroll
    for (int i = 0; i < 4; ++i) {
      f32x4 a = red[i * 64], c2 = red[(4 + i) * 64];
#pragma unroll
      for (int e = 0; e < 4; ++e) { acc0[4 * i + e] += a[e]; acc1[4 * i + e] += c2[e]; }
    }
    const unsigned r32 = lane & 31u;
    __bf16* orow = out + ((size_t)b * SQ + 32u * w + r32) * 1024u + br * 512u + nt * 64u + 16u * hh;
    *(bf16x8*)(orow) = pack8n(acc0, 0);
    *(bf16x8*)(orow + 8) = pack8n(acc0, 8);
    *(bf16x8*)(orow + 32) = pack8n(acc1, 0);
    *(bf16x8*)(orow + 40) = pack8n(acc1, 8);
  }
}

extern "C" void kernel_launch(void* const* d_in, const int* in_sizes, int n_in, void* d_out, int out_size, void* d_ws,
                              size_t ws_size, hipStream_t stream) {
  const __bf16* xA = (const __bf16*)d_in[0];
  const __bf16* kA = (const __bf16*)d_in[1];
  const __bf16* vA = (const __bf16*)d_in[2];
  const __bf16* wqA = (const __bf16*)d_in[3];
  const __bf16* woA = (const __bf16*)d_in[4];
  const __bf16* xB = (const __bf16*)d_in[5];
  const __bf16* kB = (const __bf16*)d_in[6];
  const __bf16* vB = (const __bf16*)d_in[7];
  const __bf16* wqB = (const __bf16*)d_in[8];
  const __bf16* woB = (const __bf16*)d_in[9];
  __bf16* ows = (__bf16*)d_ws;
  hipFuncSetAttribute((const void*)attn_kernel, hipFuncAttributeMaxDynamicSharedMemorySize, NSLOT * SLOT);
  hipFuncSetAttribute((const void*)oproj_kernel, hipFuncAttributeMaxDynamicSharedMemorySize, 8 * SLOT);
  hipLaunchKernelGGL(attn_kernel, dim3(512), dim3(256), NSLOT * SLOT, stream, xA, kA, vA, wqA, woA, xB, kB, vB, wqB, woB, ows);
  hipLaunchKernelGGL(oproj_kernel, dim3(256), dim3(512), 8 * SLOT, stream, (const __bf16*)ows, woA, woB, (__bf16*)d_out);
}
```

```cpp
#include <hip/hip_runtime.h>
#include <hip/hip_bf16.h>
#include <stdint.h>

typedef __attribute__((ext_vector_type(8))) __bf16 bf16x8;
typedef __attribute__((ext_vector_type(4))) __bf16 bf16x4;
typedef __attribute__((ext_vector_type(2))) __bf16 bf16x2;
typedef __attribute__((ext_vector_type(16))) float f32x16;
typedef __attribute__((ext_vector_type(2))) float f32x2;
typedef __attribute__((ext_vector_type(4))) unsigned u32x4;
#define LAS __attribute__((address_space(3)))
#define DI __device__ __forceinline__
typedef __amdgpu_buffer_rsrc_t rsrc_t;

constexpr int NB = 16, SQ = 128, DM = 512, NH = 32, SK = 512, DH = 128;
constexpr unsigned SLOT = 16384;

#define WAITV(n) asm volatile("s_waitcnt vmcnt(" #n ")" ::: "memory")

DI unsigned cvtpk(float lo, float hi) {
  f32x2 v = {lo, hi};
  bf16x2 b = __builtin_convertvector(v, bf16x2);
  return __builtin_bit_cast(unsigned, b);
}
DI bf16x8 pack8(const f32x16& a, int base, float sc) {
  u32x4 w = {cvtpk(a[base + 0] * sc, a[base + 1] * sc), cvtpk(a[base + 2] * sc, a[base + 3] * sc),
             cvtpk(a[base + 4] * sc, a[base + 5] * sc), cvtpk(a[base + 6] * sc, a[base + 7] * sc)};
  return __builtin_bit_cast(bf16x8, w);
}
DI bf16x8 pack8n(const f32x16& a, int base) {
  u32x4 w = {cvtpk(a[base + 0], a[base + 1]), cvtpk(a[base + 2], a[base + 3]),
             cvtpk(a[base + 4], a[base + 5]), cvtpk(a[base + 6], a[base + 7])};
  return __builtin_bit_cast(bf16x8, w);
}
DI bf16x8 lds_b128(unsigned addr) { return *(LAS const bf16x8*)(addr); }
DI bf16x8 lds_tr2(unsigned addr0, unsigned addr1) {
  bf16x4 lo = __builtin_amdgcn_ds_read_tr16_b64_v4bf16((LAS bf16x4*)(addr0));
  bf16x4 hi = __builtin_amdgcn_ds_read_tr16_b64_v4bf16((LAS bf16x4*)(addr1));
  return __builtin_shufflevector(lo, hi, 0, 1, 2, 3, 4, 5, 6, 7);
}
DI float xhalf_sum(float x) {
  auto r = __builtin_amdgcn_permlane32_swap(__float_as_uint(x), __float_as_uint(x), false, false);
  return __uint_as_float(r[0]) + __uint_as_float(r[1]);
}
#define MFMA(a, b, c) __builtin_amdgcn_mfma_f32_32x32x16_bf16((a), (b), (c), 0, 0, 0)

#define SB() __builtin_amdgcn_sched_barrier(0)
DI unsigned cvtpk_pin(float lo, float hi) { unsigned t = cvtpk(lo, hi); asm volatile("" : "+v"(t)); return t; }
#define PIN(x) asm volatile("" : "+v"(x))
#define PIN2(x, y) asm volatile("" : "+v"(x), "+v"(y))
#define ADD0(acc, x) acc += (x)
#define ADD1(acc, x) acc = __builtin_fmaf((x), one, acc)
template <int I, int NT> DI void dma1(unsigned m0v, unsigned v, rsrc_t r, unsigned soff) {
  if (NT) asm volatile("s_mov_b32 m0, %0\n\ts_nop 0\n\tbuffer_load_dwordx4 %1, %2, %3 offen offset:%4 nt lds"
                       :: "s"(m0v), "v"(v), "s"(r), "s"(soff), "i"(I * 1024) : "memory");
  else asm volatile("s_mov_b32 m0, %0\n\ts_nop 0\n\tbuffer_load_dwordx4 %1, %2, %3 offen offset:%4 lds"
                    :: "s"(m0v), "v"(v), "s"(r), "s"(soff), "i"(I * 1024) : "memory");
}
DI void xld(bf16x8& d, unsigned v, rsrc_t r, unsigned soff) {
  asm volatile("buffer_load_dwordx4 %0, %1, %2, %3 offen" : "=v"(d) : "v"(v), "s"(r), "s"(soff) : "memory");
}
enum { TNONE = 0, TW = 1, TK = 2, TV = 3 };

struct Ctx {
  rsrc_t rx, rw, rk, rv;
  unsigned voffKV, voffW, voffX, lbK, lbV, lbW, lbX, lwX, m0w;
  float one;
};
template <int T, int I> DI void piece(const Ctx& c, unsigned slot, unsigned so) {
  if (T == TW) dma1<I, 0>(c.m0w + slot, (c.voffW ^ ((I >> 1) << 5)) + (I >> 1) * 65536u + (I & 1) * 128u + 4096u - I * 1024u, c.rw, so);
  if (T == TK) dma1<I, 1>(c.m0w + slot, (c.voffKV ^ ((I >> 1) << 5)) + (I >> 1) * 2048u + (I & 1) * 128u + 4096u - I * 1024u, c.rk, so);
  if (T == TV) dma1<I, 1>(c.m0w + slot, (c.voffKV ^ ((I >> 1) << 5)) + (I >> 1) * 2048u + (I & 1) * 128u + 4096u - I * 1024u, c.rv, so);
}
template <int T> DI void piece_rt(const Ctx& c, int i, unsigned slot, unsigned so) {
  if (i == 0) piece<T, 0>(c, slot, so);
  if (i == 1) piece<T, 1>(c, slot, so);
  if (i == 2) piece<T, 2>(c, slot, so);
  if (i == 3) piece<T, 3>(c, slot, so);
}
template <int T> DI void tile_all(const Ctx& c, unsigned slot, unsigned so) {
  piece<T, 0>(c, slot, so); piece<T, 1>(c, slot, so); piece<T, 2>(c, slot, so); piece<T, 3>(c, slot, so);
}
DI void xld1(const Ctx& c, bf16x8 (&x)[4], int i, unsigned so) { xld(x[i], c.voffX, c.rx, so + i * 8192u); }
DI void xld_all(const Ctx& c, bf16x8 (&x)[4], unsigned so) { xld1(c, x, 0, so); xld1(c, x, 1, so); xld1(c, x, 2, so); xld1(c, x, 3, so); }
DI void xwr(unsigned lw, const bf16x8 (&x)[4]) {
#pragma unroll
  for (unsigned i = 0; i < 4; ++i) *(LAS bf16x8*)((lw ^ ((i & 1u) << 6)) + i * 1024u) = x[i];
}
DI bf16x8 xfrag(unsigned lb, unsigned ks) { return lds_b128(lb ^ (ks << 5)); }
#define TIE4(X) "+v"(X[0]), "+v"(X[1]), "+v"(X[2]), "+v"(X[3])
#define SYNCX(n, X) asm volatile("s_waitcnt vmcnt(" #n ") lgkmcnt(0)\n\ts_barrier" : TIE4(X) :: "memory")
#define SYNCX2(n, X, Y) asm volatile("s_waitcnt vmcnt(" #n ") lgkmcnt(0)\n\ts_barrier" : TIE4(X), TIE4(Y) :: "memory")
#define SYNC(n) asm volatile("s_waitcnt vmcnt(" #n ") lgkmcnt(0)\n\ts_barrier" ::: "memory")

constexpr float SMC = 0.08838834764831845f * 1.4426950408889634f;
constexpr float THR = 8.0f;
#define EXP2(S, R) S[R] = __builtin_amdgcn_exp2f(S[R])
DI bf16x8 kfrag(unsigned a0, unsigned a1, unsigned i) {
  const unsigned s = i >> 1;
  return lds_b128(((s & 1u) ? a1 : a0) + 8192u * (i & 1u) + 512u * (s >> 1));
}
DI bf16x8 vfrag(unsigned a0, unsigned a1, unsigned i) {
  const unsigned o = 4096u * (i >> 2) + 512u * (i & 3u);
  return lds_tr2(a0 + o, a1 + o + 2048u);
}
DI bf16x8 wfrag(unsigned a0, unsigned a1, unsigned i) {
  const unsigned o = 4096u * (i >> 2) + 512u * (i & 3u);
  return lds_tr2(a0 + o, a1 + o + 256u);
}

template <int T0, int T1, bool XW, bool XL>
DI void qphase(const Ctx& c, f32x16 (&acc)[4], const bf16x8 (&xb)[4], bf16x8 (&xn)[4], const bf16x8 (&xw)[4], bf16x8 (&xl)[4],
               unsigned sc, unsigned s0, unsigned so0, unsigned s1, unsigned so1, unsigned lbx, unsigned lwx, unsigned xso) {
  const unsigned a0 = c.lbW + sc, a1 = a0 ^ 16u;
  bf16x8 af[16];
  af[0] = wfrag(a0, a1, 0); af[1] = wfrag(a0, a1, 1); af[2] = wfrag(a0, a1, 2); af[3] = wfrag(a0, a1, 3);
  if (XW) xwr(lwx, xw);
  piece<T0, 0>(c, s0, so0);
  SB();
#pragma unroll
  for (unsigned i = 0; i < 16; i += 2) {
    if (i + 4 < 16) { af[i + 4] = wfrag(a0, a1, i + 4); af[i + 5] = wfrag(a0, a1, i + 5); }
    if (XW && i == 12) { xn[0] = xfrag(lbx, 0); xn[1] = xfrag(lbx, 1); }
    if (XW && i == 14) { xn[2] = xfrag(lbx, 2); xn[3] = xfrag(lbx, 3); }
    SB();
    acc[i & 3] = MFMA(af[i], xb[i >> 2], acc[i & 3]);
    SB();
    if (i < 6) piece_rt<T0>(c, (i >> 1) + 1, s0, so0);
    else if (i < 14) {
      if (XL) xld1(c, xl, (i - 6) >> 1, xso);
      if (T1 != TNONE) piece_rt<(T1 != TNONE ? T1 : TW)>(c, (i - 6) >> 1, s1, so1);
    }
    SB();
    acc[(i + 1) & 3] = MFMA(af[i + 1], xb[i >> 2], acc[(i + 1) & 3]);
    SB();
  }
}

template <bool FIN, int T0>
DI void aphase(const Ctx& c, const bf16x8 (&qf)[8], const f32x16& negm, f32x16& n0, f32x16& n1, f32x16& p0, f32x16& p1,
               u32x4 (&pw)[4], float& l, unsigned sc, unsigned s0, unsigned so0) {
  const unsigned a0 = c.lbK + sc, a1 = a0 ^ 32u;
  const float one = c.one;
  bf16x8 kf[16];
  kf[0] = kfrag(a0, a1, 0); kf[1] = kfrag(a0, a1, 1); kf[2] = kfrag(a0, a1, 2); kf[3] = kfrag(a0, a1, 3);
  float ps0 = l, ps1 = 0.f;
  piece<T0, 0>(c, s0, so0);
  if (FIN) {
#pragma unroll
    for (int e = 0; e < 8; ++e) pw[e >> 2][e & 3] = cvtpk_pin(p0[2 * e], p0[2 * e + 1]);
  }
  SB();
#pragma unroll
  for (unsigned i = 0; i < 16; i += 2) {
    if (i + 4 < 16) { kf[i + 4] = kfrag(a0, a1, i + 4); kf[i + 5] = kfrag(a0, a1, i + 5); }
    SB();
    if (i == 0) { if (FIN) n0 = MFMA(kf[i], qf[i >> 1], negm); else { f32x16 z = {}; n0 = MFMA(kf[i], qf[i >> 1], z); } }
    else n0 = MFMA(kf[i], qf[i >> 1], n0);
    SB();
    if (FIN) { EXP2(p1, i); EXP2(p1, i + 1); PIN2(p1[i], p1[i + 1]); }
    if (i < 6) piece_rt<T0>(c, (i >> 1) + 1, s0, so0);
    SB();
    if (i == 0) { if (FIN) n1 = MFMA(kf[i + 1], qf[i >> 1], negm); else { f32x16 z = {}; n1 = MFMA(kf[i + 1], qf[i >> 1], z); } }
    else n1 = MFMA(kf[i + 1], qf[i >> 1], n1);
    SB();
    if (FIN) {
      pw[2 + (i >> 3)][(i >> 1) & 3] = cvtpk_pin(p1[i], p1[i + 1]);
      ADD0(ps0, p1[i]);
      if (i == 0) ps1 = p1[1]; else ADD1(ps1, p1[i + 1]);
      PIN2(ps0, ps1);
    }
    SB();
  }
  if (FIN) l = ps0 + ps1;
}

DI float row_max(f32x16& s0, f32x16& s1) {
  float a, b;
  asm("v_max3_f32 %0, %2, %3, %4\n\tv_max3_f32 %1, %15, %16, %17\n\t"
      "v_max3_f32 %0, %0, %5, %6\n\tv_max3_f32 %1, %1, %18, %19\n\t"
      "v_max3_f32 %0, %0, %7, %8\n\tv_max3_f32 %1, %1, %20, %21\n\t"
      "v_max3_f32 %0, %0, %9, %10\n\tv_max3_f32 %1, %1, %22, %23\n\t"
      "v_max3_f32 %0, %0, %11, %12\n\tv_max3_f32 %1, %1, %24, %25\n\t"
      "v_max3_f32 %0, %0, %13, %14\n\tv_max3_f32 %1, %1, %26, %27"
      : "=&v"(a), "=&v"(b)
      : "v"(s0[0]), "v"(s0[1]), "v"(s0[2]), "v"(s0[3]), "v"(s0[4]), "v"(s0[5]), "v"(s0[6]), "v"(s0[7]), "v"(s0[8]), "v"(s0[9]),
        "v"(s0[10]), "v"(s0[11]), "v"(s0[12]),
        "v"(s1[0]), "v"(s1[1]), "v"(s1[2]), "v"(s1[3]), "v"(s1[4]), "v"(s1[5]), "v"(s1[6]), "v"(s1[7]), "v"(s1[8]), "v"(s1[9]),
        "v"(s1[10]), "v"(s1[11]), "v"(s1[12]));
  asm("v_max3_f32 %0, %0, %2, %3\n\tv_max3_f32 %1, %1, %4, %5\n\t"
      "v_max3_f32 %0, %0, %6, %7\n\tv_max_f32_e32 %0, %0, %1\n\t"
      "v_mov_b32_e32 %1, %0\n\ts_nop 1\n\tv_permlane32_swap_b32_e32 %0, %1\n\tv_max_f32_e32 %0, %0, %1"
      : "+v"(a), "+v"(b)
      : "v"(s0[13]), "v"(s0[14]), "v"(s1[13]), "v"(s1[14]), "v"(s0[15]), "v"(s1[15]));
  return a;
}

template <bool LAST, int T0, int XL>
DI void bphase(const Ctx& c, f32x16 (&o)[4], f32x16& negm, f32x16& s0, f32x16& s1, u32x4 (&pw)[4], float& l,
               unsigned sc, unsigned sl0, unsigned so0, bf16x8 (&xa)[4], bf16x8 (&xb)[4], unsigned xso, unsigned xso2) {
  const unsigned a0 = c.lbV + sc, a1 = a0 ^ 32u;
  const float one = c.one;
  bf16x8 vf[16];
  vf[0] = vfrag(a0, a1, 0); vf[1] = vfrag(a0, a1, 1); vf[2] = vfrag(a0, a1, 2); vf[3] = vfrag(a0, a1, 3);
  float ps0 = 0.f, ps1 = 0.f, alpha = 1.f;
  bool resc = false;
  if (T0 != TNONE) piece<(T0 != TNONE ? T0 : TW), 0>(c, sl0, so0);
  if (LAST) {
#pragma unroll
    for (int e = 0; e < 8; ++e) pw[e >> 2][e & 3] = cvtpk_pin(s0[2 * e], s0[2 * e + 1]);
  } else {
    asm volatile("s_nop 3" : "+v"(s0), "+v"(s1));
    const float rmax = row_max(s0, s1);
    if (__builtin_expect(__any(rmax > THR), 0)) {
      const float dl = fmaxf(rmax, 0.f);
      alpha = __builtin_amdgcn_exp2f(-dl);
      l *= alpha; resc = true;
#pragma unroll
      for (int r = 0; r < 16; ++r) { s0[r] -= dl; s1[r] -= dl; }
      asm volatile("v_sub_f32 %0, %0, %16\n\tv_sub_f32 %1, %1, %16\n\tv_sub_f32 %2, %2, %16\n\tv_sub_f32 %3, %3, %16\n\t"
                   "v_sub_f32 %4, %4, %16\n\tv_sub_f32 %5, %5, %16\n\tv_sub_f32 %6, %6, %16\n\tv_sub_f32 %7, %7, %16\n\t"
                   "v_sub_f32 %8, %8, %16\n\tv_sub_f32 %9, %9, %16\n\tv_sub_f32 %10, %10, %16\n\tv_sub_f32 %11, %11, %16\n\t"
                   "v_sub_f32 %12, %12, %16\n\tv_sub_f32 %13, %13, %16\n\tv_sub_f32 %14, %14, %16\n\tv_sub_f32 %15, %15, %16\n\ts_nop 1"
                   : "+v"(negm[0]), "+v"(negm[1]), "+v"(negm[2]), "+v"(negm[3]), "+v"(negm[4]), "+v"(negm[5]), "+v"(negm[6]), "+v"(negm[7]),
                     "+v"(negm[8]), "+v"(negm[9]), "+v"(negm[10]), "+v"(negm[11]), "+v"(negm[12]), "+v"(negm[13]), "+v"(negm[14]), "+v"(negm[15])
                   : "v"(dl));
    }
  }
  SB();
#pragma unroll
  for (unsigned i = 0; i < 16; i += 2) {
    const unsigned ks = i >> 2, db = i & 3u;
    if (i + 4 < 16) { vf[i + 4] = vfrag(a0, a1, i + 4); vf[i + 5] = vfrag(a0, a1, i + 5); }
    SB();
    o[db] = MFMA(vf[i], __builtin_bit_cast(bf16x8, pw[ks]), o[db]);
    SB();
    if (i < 6) { if (T0 != TNONE) piece_rt<(T0 != TNONE ? T0 : TW)>(c, (i >> 1) + 1, sl0, so0); }
    else if (i < 14 && XL >= 1) xld1(c, xa, (i - 6) >> 1, xso);
    if (LAST) {
      if (i < 8) {
        EXP2(s1, 2 * i); EXP2(s1, 2 * i + 1);
        pw[2 + (i >> 2)][i & 3] = cvtpk_pin(s1[2 * i], s1[2 * i + 1]);
        if (i == 0) { ps0 = s1[0]; ps1 = s1[1]; } else { ADD0(ps0, s1[2 * i]); ADD1(ps1, s1[2 * i + 1]); }
        PIN2(ps0, ps1);
      }
    } else {
      EXP2(s0, i); EXP2(s0, i + 1); PIN2(s0[i], s0[i + 1]);
    }
    SB();
    o[db + 1] = MFMA(vf[i + 1], __builtin_bit_cast(bf16x8, pw[ks]), o[db + 1]);
    SB();
    if (i >= 6 && i < 14 && XL >= 2) xld1(c, xb, (i - 6) >> 1, xso2);
    if (LAST) {
      if (i < 8) {
        EXP2(s1, 2 * i + 2); EXP2(s1, 2 * i + 3);
        pw[2 + (i >> 2)][(i & 3) + 1] = cvtpk_pin(s1[2 * i + 2], s1[2 * i + 3]);
        ADD0(ps0, s1[2 * i + 2]); ADD1(ps1, s1[2 * i + 3]);
        PIN2(ps0, ps1);
      }
    } else {
      if (i == 0) { ps0 = s0[0]; ps1 = s0[1]; } else { ADD0(ps0, s0[i]); ADD1(ps1, s0[i + 1]); }
      PIN2(ps0, ps1);
    }
    SB();
  }
  l += ps0 + ps1;
  if (!LAST) {
    if (__builtin_expect(resc, 0)) {
#pragma unroll
      for (int db = 0; db < 4; ++db)
#pragma unroll
        for (int r = 0; r < 16; ++r) o[db][r] *= alpha;
    }
  }
}

struct GCtx {
  rsrc_t rx, rw, rwo, ro;
  unsigned voffW, voffX, voffWo, lbW, lbA, lbX, lwX, m0w, vo;
};
template <int I> DI void gw_piece(const GCtx& g, unsigned slot, unsigned so) {
  dma1<I, 0>(g.m0w + slot, (g.voffW ^ ((I >> 1) << 5)) + (I >> 1) * 65536u + (I & 1) * 128u + 4096u - I * 1024u, g.rw, so);
}
template <int I> DI void go_piece(const GCtx& g, unsigned slot, unsigned so) { dma1<I, 0>(g.m0w + slot, g.voffWo + I * 7168u, g.rwo, so); }
DI void g_piece(const GCtx& g, bool isw, int i, unsigned slot, unsigned so) {
  if (isw) {
    if (i == 0) gw_piece<0>(g, slot, so);
    if (i == 1) gw_piece<1>(g, slot, so);
    if (i == 2) gw_piece<2>(g, slot, so);
    if (i == 3) gw_piece<3>(g, slot, so);
  } else {
    if (i == 0) go_piece<0>(g, slot, so);
    if (i == 1) go_piece<1>(g, slot, so);
    if (i == 2) go_piece<2>(g, slot, so);
    if (i == 3) go_piece<3>(g, slot, so);
  }
}
DI void gxld1(const GCtx& g, bf16x8 (&x)[4], int i, unsigned so) { xld(x[i], g.voffX, g.rx, so + i * 8192u); }
DI void gxld_all(const GCtx& g, bf16x8 (&x)[4], unsigned so) { gxld1(g, x, 0, so); gxld1(g, x, 1, so); gxld1(g, x, 2, so); gxld1(g, x, 3, so); }
template <int IMM> DI void ldo(bf16x8& d, unsigned voff, rsrc_t r, unsigned so) {
  asm volatile("buffer_load_dwordx4 %0, %1, %2, %3 offen offset:%4 sc1" : "=v"(d) : "v"(voff), "s"(r), "s"(so), "i"(IMM) : "memory");
}
DI void ldo_rt(const GCtx& g, bf16x8 (&S)[8], int i, unsigned so) {
  if (i == 0) ldo<0>(S[0], g.vo, g.ro, so);
  if (i == 1) ldo<1024>(S[1], g.vo, g.ro, so);
  if (i == 2) ldo<2048>(S[2], g.vo, g.ro, so);
  if (i == 3) ldo<3072>(S[3], g.vo, g.ro, so);
  if (i == 4) ldo<0>(S[4], g.vo, g.ro, so + 4096u);
  if (i == 5) ldo<1024>(S[5], g.vo, g.ro, so + 4096u);
  if (i == 6) ldo<2048>(S[6], g.vo, g.ro, so + 4096u);
  if (i == 7) ldo<3072>(S[7], g.vo, g.ro, so + 4096u);
}
#define TIE8(S) "+v"(S[0]), "+v"(S[1]), "+v"(S[2]), "+v"(S[3]), "+v"(S[4]), "+v"(S[5]), "+v"(S[6]), "+v"(S[7])
#define SYNCO(n, S) asm volatile("s_waitcnt vmcnt(" #n ") lgkmcnt(0)\n\ts_barrier" : TIE8(S) :: "memory")

template <int P, bool LD, int XL>
DI void ophase(const GCtx& g, f32x16& acc0, f32x16& acc1, const bf16x8 (&SC)[8], bf16x8 (&SN)[8], bf16x8 (&xl)[4], bf16x8 (&xl2)[4],
               bool isw, unsigned so0, unsigned oso, unsigned xso, unsigned xso2) {
  constexpr unsigned SCO = (P & 3) * SLOT, SI = ((P + 3) & 3) * SLOT;
  bf16x8 f[16];
  const unsigned a0 = g.lbA + SCO, a1 = (g.lbA ^ 64u) + SCO;
#define OFRAG(i) lds_tr2((((i) & 1) ? a1 : a0) + 2048u * ((i) >> 1), (((i) & 1) ? a1 : a0) + 2048u * ((i) >> 1) + 1024u)
  f[0] = OFRAG(0); f[1] = OFRAG(1); f[2] = OFRAG(2); f[3] = OFRAG(3);
  if (P >= 5) {
    if (isw) { gw_piece<0>(g, SI, so0); gw_piece<1>(g, SI, so0); gw_piece<2>(g, SI, so0); gw_piece<3>(g, SI, so0); }
    else { go_piece<0>(g, SI, so0); go_piece<1>(g, SI, so0); go_piece<2>(g, SI, so0); go_piece<3>(g, SI, so0); }
  }
  SB();
#pragma unroll
  for (unsigned i = 0; i < 16; i += 2) {
    if (i + 4 < 16) { f[i + 4] = OFRAG(i + 4); f[i + 5] = OFRAG(i + 5); }
    SB();
    acc0 = MFMA(f[i], SC[i >> 1], acc0);
    SB();
    if (P < 5 && i < 8) g_piece(g, false, i >> 1, SI, so0);
    if (XL >= 1 && i < 8) gxld1(g, xl, i >> 1, xso);
    if (XL >= 2 && i >= 8) gxld1(g, xl2, (i - 8) >> 1, xso2);
    SB();
    acc1 = MFMA(f[i + 1], SC[i >> 1], acc1);
    SB();
    if (LD) ldo_rt(g, SN, i >> 1, oso);
    SB();
  }
#undef OFRAG
}
typedef unsigned long long u64;
DI void poll_stage(const u64* mk, unsigned lane, u64 token) {
  for (unsigned spin = 0; spin < (1u << 20); ++spin) {
    const u64 v = __hip_atomic_load(mk + (lane & 7u), __ATOMIC_RELAXED, __HIP_MEMORY_SCOPE_AGENT);
    if (__all(v == token)) break;
    __builtin_amdgcn_s_sleep(4);
  }
  asm volatile("" ::: "memory");
}
#define BAR() asm volatile("s_waitcnt lgkmcnt(0)\n\ts_barrier" ::: "memory")

template <int IMM> DI void st16(unsigned voff, rsrc_t r, unsigned so, const bf16x8& d) {
  asm volatile("buffer_store_dwordx4 %0, %1, %2, %3 offen offset:%4 sc1\n\ts_nop 1" :: "v"(d), "v"(voff), "s"(r), "s"(so), "i"(IMM) : "memory");
}
DI void st8(unsigned vo, rsrc_t r, unsigned so, const bf16x8 (&f)[8]) {
  st16<0>(vo, r, so, f[0]); st16<1024>(vo, r, so, f[1]); st16<2048>(vo, r, so, f[2]); st16<3072>(vo, r, so, f[3]);
  st16<0>(vo, r, so + 4096u, f[4]); st16<1024>(vo, r, so + 4096u, f[5]); st16<2048>(vo, r, so + 4096u, f[6]); st16<3072>(vo, r, so + 4096u, f[7]);
}
DI void ld8(unsigned vo, rsrc_t r, unsigned so, bf16x8 (&f)[8]) {
  ldo<0>(f[0], vo, r, so); ldo<1024>(f[1], vo, r, so); ldo<2048>(f[2], vo, r, so); ldo<3072>(f[3], vo, r, so);
  ldo<0>(f[4], vo, r, so + 4096u); ldo<1024>(f[5], vo, r, so + 4096u); ldo<2048>(f[6], vo, r, so + 4096u); ldo<3072>(f[7], vo, r, so + 4096u);
}

__global__ __launch_bounds__(512, 2) void fused_kernel(
    const __bf16* __restrict__ xA, const __bf16* __restrict__ kA, const __bf16* __restrict__ vA, const __bf16* __restrict__ wqA,
    const __bf16* __restrict__ woA,
    const __bf16* __restrict__ xB, const __bf16* __restrict__ kB, const __bf16* __restrict__ vB, const __bf16* __restrict__ wqB,
    const __bf16* __restrict__ woB, __bf16* __restrict__ ows, u64* __restrict__ sync, __bf16* __restrict__ out) {
  extern __shared__ __attribute__((aligned(16))) char smem[];
  const unsigned tid = threadIdx.x, lane = tid & 63u;
  const unsigned w8 = __builtin_amdgcn_readfirstlane(tid >> 6);
  const unsigned w = w8 & 3u, gq = w8 >> 2;
  constexpr unsigned GRING = 5u * SLOT;
  const unsigned lds0 = (unsigned)(size_t)(LAS char*)smem;
  const unsigned ldsb = lds0 + gq * GRING;
  const unsigned bid = blockIdx.x;
  const unsigned xcd = bid & 7u, idx = bid >> 3;
  const unsigned pair = xcd * 4u + (idx >> 3), sub = idx & 7u;
  const unsigned br = pair >> 4, b = pair & 15u;
  const unsigned h0 = 8u * gq + sub;
  constexpr unsigned HT = 131072u, OT = 32768u;
  const unsigned vo = w * 8192u + lane * 16u;
  const rsrc_t ro = __builtin_amdgcn_make_buffer_rsrc((void*)((const char*)ows + (size_t)pair * NH * OT), 0, NH * OT, 0x00020000);
  u64* ep = sync + pair * 64u;
  u64 token;
  asm volatile("global_load_dwordx2 %0, %1, %2 sc1" : "=v"(token) : "v"(0u), "s"(ep) : "memory");
  Ctx c;
  c.rx = __builtin_amdgcn_make_buffer_rsrc((void*)((br ? xB : xA) + (size_t)b * SQ * DM), 0, SQ * DM * 2, 0x00020000);
  c.rw = __builtin_amdgcn_make_buffer_rsrc((void*)((const char*)(br ? wqB : wqA) - 4096), 0, DM * NH * DH * 2 + 4096, 0x00020000);
  c.rk = __builtin_amdgcn_make_buffer_rsrc((void*)((const char*)(br ? kB : kA) - 4096), 0, NB * NH * SK * DH * 2 + 4096, 0x00020000);
  c.rv = __builtin_amdgcn_make_buffer_rsrc((void*)((const char*)(br ? vB : vA) - 4096), 0, NB * NH * SK * DH * 2 + 4096, 0x00020000);
  const unsigned l4 = lane >> 4;
  const unsigned r32 = lane & 31u, hh = lane >> 5;
  const unsigned q4 = (lane >> 2) & 3u, p4 = lane & 3u, dhalf = (lane >> 4) & 1u;
  {
    const unsigned row = 16u * w + ((lane >> 2) & 7u), col = hh * 64u + ((p4 ^ dhalf) << 4);
    c.voffKV = row * 256u + col;
    c.voffW = row * 8192u + col;
  }
  c.voffX = (32u * w + (lane >> 3)) * 1024u + ((lane & 7u) << 4);
  c.lbK = ldsb + 2048u * (r32 >> 3) + 64u * (r32 & 7u) + ((hh ^ ((r32 >> 2) & 3u)) << 4);
  c.lbV = ldsb + 64u * (4u * hh + q4) + (((2u * dhalf + (p4 >> 1)) ^ hh) << 4) + 8u * (p4 & 1u);
  c.lbW = ldsb + 2048u * hh + 64u * q4 + (((2u * dhalf + (p4 & 1u)) ^ (2u * hh)) << 4) + 8u * (p4 >> 1);
  c.lbX = ldsb + w * 4096u + r32 * 128u + ((hh ^ ((r32 >> 1) & 7u)) << 4);
  c.lwX = ldsb + w * 4096u + (lane >> 3) * 128u + (((lane & 7u) ^ l4) << 4);
  c.m0w = ldsb + w * 4096u;
  c.one = 1.0f;
  asm volatile("" : "+v"(c.one));
  const float one = c.one;
  constexpr unsigned WSTEP = 64u * 8192u, OOB = 0x7f000000u, KT = 16384u;

  const unsigned rot = (idx >> 3) * 2u + (sub & 1u);
#define WOFF(k) ((((k) + rot) & 7u) * WSTEP)
#define XOFF(k) ((((k) + rot) & 7u) * 128u)
  bf16x8 r0[4], r1[4], r2[4];
  unsigned f0 = 0, f1 = SLOT, f2 = 2 * SLOT, f3 = 3 * SLOT, xs = 4 * SLOT;
  tile_all<TW>(c, f0, h0 * 256u + WOFF(0));
  tile_all<TW>(c, f1, h0 * 256u + WOFF(1));
  tile_all<TW>(c, f2, h0 * 256u + WOFF(2)); xld_all(c, r0, XOFF(0)); xld_all(c, r1, XOFF(1));
  xld_all(c, r2, XOFF(2));

  unsigned dummy = 0;
  if (gq == 1u) { for (int i = 0; i < 8; ++i) BAR(); }
  for (unsigned unit = 0; unit < 2; ++unit) {
    const unsigned h = h0 + 16u * unit;
    const unsigned xso = 0u, wh = h * 256u;
    const unsigned kv = (b * NH + h) * (SK * DH * 2);
    const unsigned xnx = unit == 0 ? 0u : OOB;
    const unsigned wnx = unit == 0 ? wh + 4096u : OOB;
    bf16x8 qf[8];
    {
      f32x16 acc[4] = {};
      bf16x8 fa[4], fb[4];
      const unsigned lbx = c.lbX + xs, lwx = c.lwX + xs;
      if (unit == 0) WAITV(4);
      SYNCX2(13, r0, r1);
      PIN(token);
      xwr(lwx, r0);
      fa[0] = xfrag(lbx, 0); fa[1] = xfrag(lbx, 1); fa[2] = xfrag(lbx, 2); fa[3] = xfrag(lbx, 3);
      qphase<TW, TNONE, true, true>(c, acc, fa, fb, r1, r0, f0, f3, wh + WOFF(3u), 0, 0, lbx, lwx, xso + XOFF(3u));
      if (unit == 0) WAITV(8);
      SYNCX(17, r2);
      qphase<TW, TNONE, true, true>(c, acc, fb, fa, r2, r1, f1, f0, wh + WOFF(4u), 0, 0, lbx, lwx, xso + XOFF(4u));
      SYNCX(8, r0);
      PIN(dummy);
      if (unit == 1u && lane == 0 && w == 0) __hip_atomic_store(ep + 8u + 8u * gq + sub, token + 1ull, __ATOMIC_RELAXED, __HIP_MEMORY_SCOPE_AGENT);
      qphase<TW, TNONE, true, true>(c, acc, fa, fb, r0, r2, f2, f1, wh + WOFF(5u), 0, 0, lbx, lwx, xso + XOFF(5u));
      SYNCX(8, r1);
      qphase<TW, TNONE, true, true>(c, acc, fb, fa, r1, r0, f3, f2, wh + WOFF(6u), 0, 0, lbx, lwx, xso + XOFF(6u));
      SYNCX(8, r2);
      qphase<TW, TNONE, true, true>(c, acc, fa, fb, r2, r1, f0, f3, wh + WOFF(7u), 0, 0, lbx, lwx, xso + XOFF(7u));
      SYNCX(8, r0);
      qphase<TK, TNONE, true, false>(c, acc, fb, fa, r0, r2, f1, f0, kv, 0, 0, lbx, lwx, 0);
      SYNCX(4, r1);
      qphase<TK, TNONE, true, false>(c, acc, fa, fb, r1, r2, f2, f1, kv + KT, 0, 0, lbx, lwx, 0);
      SYNC(12);
      qphase<TV, TK, false, false>(c, acc, fb, fa, r1, r2, f3, f2, kv, xs, kv + 2u * KT, lbx, lwx, 0);
#pragma unroll
      for (int db = 0; db < 4; ++db) { qf[2 * db] = pack8(acc[db], 0, SMC); qf[2 * db + 1] = pack8(acc[db], 8, SMC); }
    }
    unsigned g0 = f0, g1 = f1, g2 = f2, g3 = xs, g4 = f3;
    f32x16 o[4] = {};
    f32x16 sA0, sA1, sB0, sB1, negm;
    u32x4 pw[4];
    float l = 0.f;
    SYNC(12);
    aphase<false, TV>(c, qf, negm, sA0, sA1, sB0, sB1, pw, l, g0, g4, kv + KT);
    {
      asm volatile("s_nop 7\n\ts_nop 7" : "+v"(sA0), "+v"(sA1));
      const float m = row_max(sA0, sA1);
      float ps0 = 0.f, ps1 = 0.f;
#pragma unroll
      for (int r = 0; r < 16; ++r) { sA0[r] -= m; sA1[r] -= m; negm[r] = -m; }
      asm volatile("" : "+v"(negm));
#pragma unroll
      for (int r = 0; r < 16; r += 2) { EXP2(sA0, r); EXP2(sA0, r + 1); ADD0(ps0, sA0[r]); ADD1(ps1, sA0[r + 1]); }
      l = ps0 + ps1;
    }
    SYNC(12);
    aphase<true, TK>(c, qf, negm, sB0, sB1, sA0, sA1, pw, l, g1, g0, kv + 3u * KT);
    SYNC(12);
    bphase<false, TV, 0>(c, o, negm, sB0, sB1, pw, l, g2, g1, kv + 2u * KT, r0, r1, 0, 0);
    { const unsigned t0 = g0, t1 = g1, t2 = g2; g0 = g3; g1 = g4; g2 = t0; g3 = t1; g4 = t2; }
    for (unsigned t = 2; t < 6; t += 2) {
      SYNC(12);
      aphase<true, TK>(c, qf, negm, sA0, sA1, sB0, sB1, pw, l, g0, g4, kv + (t + 2u) * KT);
      SYNC(12);
      bphase<false, TV, 0>(c, o, negm, sA0, sA1, pw, l, g1, g0, kv + (t + 1u) * KT, r0, r1, 0, 0);
      SYNC(12);
      aphase<true, TK>(c, qf, negm, sB0, sB1, sA0, sA1, pw, l, g2, g1, kv + (t + 3u) * KT);
      SYNC(12);
      bphase<false, TV, 0>(c, o, negm, sB0, sB1, pw, l, g3, g2, kv + (t + 2u) * KT, r0, r1, 0, 0);
      { const unsigned t4 = g4; g4 = g3; g3 = g2; g2 = g1; g1 = g0; g0 = t4; }
    }
    SYNC(12);
    aphase<true, TV>(c, qf, negm, sA0, sA1, sB0, sB1, pw, l, g0, g4, kv + 7u * KT);
    SYNC(12);
    bphase<false, TW, 0>(c, o, negm, sA0, sA1, pw, l, g1, g0, wnx + WOFF(0), r0, r1, 0, 0);
    SYNC(12);
    aphase<true, TW>(c, qf, negm, sB0, sB1, sA0, sA1, pw, l, g2, g1, wnx + WOFF(1));
    SYNC(12);
    bphase<false, TW, 2>(c, o, negm, sB0, sB1, pw, l, g3, g2, wnx + WOFF(2), r0, r1, xnx + XOFF(0), xnx + XOFF(1));
    SYNC(20);
    bphase<true, TNONE, 1>(c, o, negm, sB0, sB1, pw, l, g4, 0, 0, r2, r2, xnx + XOFF(2), 0);
    f0 = g0; f1 = g1; f2 = g2; f3 = g4; xs = g3;
    {
      const rsrc_t rwo = __builtin_amdgcn_make_buffer_rsrc((void*)(br ? woB : woA), 0, NH * DH * DM * 2, 0x00020000);
      asm volatile("buffer_load_dword %0, %1, %2, %3 offen" : "=v"(dummy)
                   : "v"(r32 * 128u), "s"(rwo), "s"(xnx + ((idx * 4u + (xcd & 3u)) * 8u + w8) * 4096u) : "memory");
    }
    const float inv = 1.0f / xhalf_sum(l);
    bf16x8 of[8];
#pragma unroll
    for (int db = 0; db < 4; ++db) { of[2 * db] = pack8(o[db], 0, inv); of[2 * db + 1] = pack8(o[db], 8, inv); }
    st8(vo, ro, h * OT, of);
  }
  asm volatile("s_waitcnt vmcnt(0) lgkmcnt(0)\n\ts_barrier" : "+v"(dummy), TIE4(r0), TIE4(r1), TIE4(r2) :: "memory");
  token += 1ull;
  if (lane == 0 && w == 0) __hip_atomic_store(ep + 8u + 8u * (2u + gq) + sub, token, __ATOMIC_RELAXED, __HIP_MEMORY_SCOPE_AGENT);

  {
    GCtx g;
    g.rwo = __builtin_amdgcn_make_buffer_rsrc((void*)((br ? woB : woA) + sub * 64u), 0, ((NH * DH - 1) * DM + 64) * 2, 0x00020000);
    g.ro = ro;
    g.rx = g.ro; g.rw = g.ro; g.voffW = 0; g.voffX = 0; g.lbW = 0; g.lbX = 0; g.lwX = 0;
    g.voffWo = (32u * w + (lane >> 3)) * 1024u + (((lane & 7u) ^ (dhalf << 2)) << 4);
    g.lbA = ldsb + (4u * hh + q4) * 128u + (((q4 >> 1) & 1u) << 6) + 2u * (16u * (p4 & 1u) + 8u * dhalf + 4u * (p4 >> 1));
    g.m0w = ldsb + w * 4096u;
    g.vo = w * 8192u + lane * 16u;
#define HD(s) (gq == 0u ? ((s) < 12u ? (s) : ((s) < 16u ? (s) + 4u : (s) + 8u)) : ((s) < 4u ? (s) + 12u : ((s) < 8u ? (s) + 16u : (s) + 20u)))
    const unsigned NS = gq == 0u ? 20u : 12u;
    f32x16 oacc0 = {}, oacc1 = {};
    bf16x8 B0[8], B1[8], B2[8], B3[8], XD[4];
#pragma unroll
    for (int i = 0; i < 4; ++i) g_piece(g, false, i, 0, HD(0u) * HT);
#pragma unroll
    for (int i = 0; i < 4; ++i) g_piece(g, false, i, SLOT, HD(1u) * HT);
#pragma unroll
    for (int i = 0; i < 4; ++i) g_piece(g, false, i, 2 * SLOT, HD(2u) * HT);
    if (gq == 0u) poll_stage(ep + 8u, lane, token);
    poll_stage(ep + 16u, lane, token);
    if (gq == 1u) poll_stage(ep + 24u, lane, token);
#pragma unroll
    for (int i = 0; i < 8; ++i) ldo_rt(g, B0, i, HD(0u) * OT);
#pragma unroll
    for (int i = 0; i < 8; ++i) ldo_rt(g, B1, i, HD(1u) * OT);
#pragma unroll
    for (int i = 0; i < 8; ++i) ldo_rt(g, B2, i, HD(2u) * OT);
    SYNCO(16, B0);
    ophase<0, true, 0>(g, oacc0, oacc1, B0, B3, XD, XD, false, HD(3u) * HT, HD(3u) * OT, 0, 0);
    SYNCO(20, B1);
    ophase<1, true, 0>(g, oacc0, oacc1, B1, B0, XD, XD, false, HD(4u) * HT, HD(4u) * OT, 0, 0);
    SYNCO(24, B2);
    ophase<2, true, 0>(g, oacc0, oacc1, B2, B1, XD, XD, false, HD(5u) * HT, HD(5u) * OT, 0, 0);
    SYNCO(24, B3);
    ophase<3, true, 0>(g, oacc0, oacc1, B3, B2, XD, XD, false, HD(6u) * HT, HD(6u) * OT, 0, 0);
    for (unsigned s = 4; s + 4u < NS; s += 4) {
      SYNCO(24, B0);
      ophase<0, true, 0>(g, oacc0, oacc1, B0, B3, XD, XD, false, HD(s + 3u) * HT, HD(s + 3u) * OT, 0, 0);
      SYNCO(24, B1);
      if (HD(s + 4u) >= 16u) poll_stage(ep + 8u + 8u * (HD(s + 4u) >> 3), lane, token);
      ophase<1, true, 0>(g, oacc0, oacc1, B1, B0, XD, XD, false, HD(s + 4u) * HT, HD(s + 4u) * OT, 0, 0);
      SYNCO(24, B2);
      ophase<2, true, 0>(g, oacc0, oacc1, B2, B1, XD, XD, false, HD(s + 5u) * HT, HD(s + 5u) * OT, 0, 0);
      SYNCO(24, B3);
      ophase<3, true, 0>(g, oacc0, oacc1, B3, B2, XD, XD, false, HD(s + 6u) * HT, HD(s + 6u) * OT, 0, 0);
    }
    SYNCO(24, B0);
    ophase<0, true, 0>(g, oacc0, oacc1, B0, B3, XD, XD, false, HD(NS - 1u) * HT, HD(NS - 1u) * OT, 0, 0);
    SYNCO(24, B1);
    ophase<1, false, 0>(g, oacc0, oacc1, B1, B0, XD, XD, false, OOB, 0, 0, 0);
    SYNCO(16, B2);
    ophase<2, false, 0>(g, oacc0, oacc1, B2, B0, XD, XD, false, OOB, 0, 0, 0);
    SYNCO(8, B3);
    ophase<3, false, 0>(g, oacc0, oacc1, B3, B0, XD, XD, false, OOB, 0, 0, 0);
    if (sub == 0u && tid == 0u) __hip_atomic_store(ep, token, __ATOMIC_RELAXED, __HIP_MEMORY_SCOPE_AGENT);
    SYNC(0);
    typedef __attribute__((ext_vector_type(4))) float f32x4;
    LAS f32x4* red = (LAS f32x4*)(lds0 + w * 8192u) + lane;
    if (gq == 1) {
#pragma unroll
      for (int i = 0; i < 4; ++i) {
        red[i * 64] = f32x4{oacc0[4 * i], oacc0[4 * i + 1], oacc0[4 * i + 2], oacc0[4 * i + 3]};
        red[(4 + i) * 64] = f32x4{oacc1[4 * i], oacc1[4 * i + 1], oacc1[4 * i + 2], oacc1[4 * i + 3]};
      }
    }
    BAR();
    if (gq == 0) {
#pragma unroll
      for (int i = 0; i < 4; ++i) {
        f32x4 a = red[i * 64], c2 = red[(4 + i) * 64];
#pragma unroll
        for (int e = 0; e < 4; ++e) { oacc0[4 * i + e] += a[e]; oacc1[4 * i + e] += c2[e]; }
      }
      __bf16* orow = out + ((size_t)b * SQ + 32u * w + r32) * 1024u + br * 512u + sub * 64u + 16u * hh;
      *(bf16x8*)(orow) = pack8n(oacc0, 0);
      *(bf16x8*)(orow + 8) = pack8n(oacc0, 8);
      *(bf16x8*)(orow + 32) = pack8n(oacc1, 0);
      *(bf16x8*)(orow + 40) = pack8n(oacc1, 8);
    }
  }
}

extern "C" void kernel_launch(void* const* d_in, const int* in_sizes, int n_in, void* d_out, int out_size, void* d_ws,
                              size_t ws_size, hipStream_t stream) {
  const __bf16* xA = (const __bf16*)d_in[0];
  const __bf16* kA = (const __bf16*)d_in[1];
  const __bf16* vA = (const __bf16*)d_in[2];
  const __bf16* wqA = (const __bf16*)d_in[3];
  const __bf16* woA = (const __bf16*)d_in[4];
  const __bf16* xB = (const __bf16*)d_in[5];
  const __bf16* kB = (const __bf16*)d_in[6];
  const __bf16* vB = (const __bf16*)d_in[7];
  const __bf16* wqB = (const __bf16*)d_in[8];
  const __bf16* woB = (const __bf16*)d_in[9];
  u64* sync = (u64*)d_ws;
  __bf16* ows = (__bf16*)((char*)d_ws + 65536);
  (void)hipFuncSetAttribute((const void*)fused_kernel, hipFuncAttributeMaxDynamicSharedMemorySize, 10 * SLOT);
  hipLaunchKernelGGL(fused_kernel, dim3(256), dim3(512), 10 * SLOT, stream, xA, kA, vA, wqA, woA, xB, kB, vB, wqB, woB, ows,
                     sync, (__bf16*)d_out);
}
```
